```python
import torch

def _softmax(x, axis=-1):
    return torch.softmax(x, dim=axis)

def _gelu(x):
    return torch.nn.functional.gelu(x, approximate='tanh')

def _silu(x):
    return torch.nn.functional.silu(x)

def _sigmoid(x):
    return torch.sigmoid(x)

def _layernorm(x, axis=-1, eps=1e-05):
    m = x.mean(dim=axis, keepdim=True)
    v = x.var(dim=axis, keepdim=True, unbiased=False)
    return (x - m) / torch.sqrt(v + eps)

def _scatter_add(tgt, idx, src, axis=0):
    return tgt.clone().index_add_(axis, idx, src)

def _segment_sum(src, seg, n):
    out = torch.zeros((n,) + src.shape[1:], dtype=src.dtype)
    return out.index_add_(0, seg, src)

def _pad(x, pw):
    flat = tuple((v for pair in reversed(pw) for v in pair))
    return torch.nn.functional.pad(x, flat)
kernel_name = 'adaLN_cond__gqa_block_cached__mixer_channel_mlp__decode'

def setup_inputs():

    _w = lambda a: (lambda t: t.to(torch.bfloat16) if t.is_floating_point() else t)(torch.as_tensor(a))

    def _A_setup():
        B, Sq, D = (1, 1, 1024)
        Hq, Hkv, Sk, Dh = (32, 4, 65536, 128)
        assert Hq % Hkv == 0, (Hq, Hkv)
        return {'x': _w((lambda *s: torch.randn(*s, dtype=torch.bfloat16) * 0.1)(B, Sq, D)), 'k_cache': _w((lambda *s: torch.randn(*s, dtype=torch.bfloat16) * 0.8)(B, Hkv, Sk, Dh)), 'v_cache': _w((lambda *s: torch.randn(*s, dtype=torch.bfloat16) * 0.8)(B, Hkv, Sk, Dh)), 'wq': _w((lambda *s: torch.randn(*s, dtype=torch.bfloat16) * 0.8)(D, Hq * Dh) * D ** (-0.5)), 'wo': _w((lambda *s: torch.randn(*s, dtype=torch.bfloat16) * 0.8)(Hq * Dh, D) * (Hq * Dh) ** (-0.5))}
    _A_in = _A_setup()

    def _B_setup():
        B, S, D, F = (1, 1, 1024, 3584)
        return {'x': _w((lambda *s: torch.randn(*s, dtype=torch.bfloat16) * 0.1)(B, S, D)), 'w1': _w((lambda *s: torch.randn(*s, dtype=torch.bfloat16) * 3.2)(D, F) * D ** (-0.5)), 'b1': _w((lambda *s: torch.randn(*s, dtype=torch.bfloat16) * 3.2)(F)), 'w2': _w((lambda *s: torch.randn(*s, dtype=torch.bfloat16) * 3.2)(F, D) * F ** (-0.5)), 'b2': _w((lambda *s: torch.randn(*s, dtype=torch.bfloat16) * 3.2)(D))}
    _B_in = _B_setup()
    return {'A_x': _A_in['x'], 'A_k_cache': _A_in['k_cache'], 'A_v_cache': _A_in['v_cache'], 'A_wq': _A_in['wq'], 'A_wo': _A_in['wo'], 'B_x': _B_in['x'], 'B_w1': _B_in['w1'], 'B_b1': _B_in['b1'], 'B_w2': _B_in['w2'], 'B_b2': _B_in['b2'], 'c': _w((lambda *s: torch.randn(*s, dtype=torch.bfloat16) * 0.4)(1, 1024) * 8.0), 'w_mod': _w((lambda *s: torch.randn(*s, dtype=torch.bfloat16) * 0.4)(1024, 6 * 1024) * 1024 ** (-0.5) * 8.0)}

def reference(A_x, A_k_cache, A_v_cache, A_wq, A_wo, B_x, B_w1, B_b1, B_w2, B_b2, c, w_mod):

    def _A_ref(x, k_cache, v_cache, wq, wo):
        B, Sq, D = x.shape
        Hq, Dh = (32, 128)
        Hkv = k_cache.shape[1]
        rep = Hq // Hkv
        q = (lambda x, *d: x.permute(*d))((x @ wq).reshape(B, Sq, Hq, Dh), 0, 2, 1, 3)
        k = (lambda x, n, axis: torch.repeat_interleave(x, n, dim=axis))(k_cache, rep, axis=1)
        v = (lambda x, n, axis: torch.repeat_interleave(x, n, dim=axis))(v_cache, rep, axis=1)
        scores = torch.einsum('bhqd,bhkd->bhqk', q, k) * Dh ** (-0.5)
        p = _softmax(scores, axis=-1)
        o = torch.einsum('bhqk,bhkd->bhqd', p, v)
        return (lambda x, *d: x.permute(*d))(o, 0, 2, 1, 3).reshape(B, Sq, Hq * Dh) @ wo

    def _B_ref(x, w1, b1, w2, b2):
        y = _layernorm(x, axis=-1)
        h = _gelu(torch.einsum('bsd,df->bsf', y, w1) + b1)
        return torch.einsum('bsf,fd->bsd', h, w2) + b2
    x = A_x
    mod = (_silu(c) @ w_mod).reshape(c.shape[0], 6, 1, -1)
    sa, ca, ga, sb, cb, gb = (mod[:, 0].clone(), mod[:, 1].clone(), mod[:, 2].clone(), mod[:, 3].clone(), mod[:, 4].clone(), mod[:, 5].clone())
    h = _layernorm(x, axis=-1) * (1 + ca) + sa
    x = x + ga * _A_ref(h, A_k_cache, A_v_cache, A_wq, A_wo)
    h = _layernorm(x, axis=-1) * (1 + cb) + sb
    x = x + gb * _B_ref(h, B_w1, B_b1, B_w2, B_b2)
    return x
```

```cpp
#include <hip/hip_runtime.h>
#include <hip/hip_bf16.h>
#include <stdint.h>

typedef unsigned short u16;
typedef unsigned v4u __attribute__((ext_vector_type(4)));
typedef float f32x4 __attribute__((ext_vector_type(4)));
typedef short bf16x8 __attribute__((ext_vector_type(8)));

__device__ __forceinline__ float bf2f(unsigned b) { return __uint_as_float(b << 16); }
__device__ __forceinline__ unsigned f2bf(float f) { return (unsigned)__bfloat16_as_ushort(__float2bfloat16(f)); }
__device__ __forceinline__ float rbf(float f) { return bf2f(f2bf(f)); }

constexpr int D = 1024, HQ = 32, HKV = 4, SK = 65536, DH = 128, F = 3584;

__device__ float block_sum(float v, float* red) {
  for (int o = 32; o > 0; o >>= 1) v += __shfl_xor(v, o);
  __syncthreads();
  if ((threadIdx.x & 63) == 0) red[threadIdx.x >> 6] = v;
  __syncthreads();
  float s = 0.f;
  for (int i = 0; i < (int)(blockDim.x >> 6); ++i) s += red[i];
  return s;
}
__device__ void ln_inplace(float* v, float* red) {
  float s = 0.f;
  for (int i = threadIdx.x; i < D; i += 256) s += v[i];
  float mean = block_sum(s, red) * (1.f / D);
  float q = 0.f;
  for (int i = threadIdx.x; i < D; i += 256) { float d = v[i] - mean; q += d * d; }
  float var = block_sum(q, red) * (1.f / D);
  float mb = rbf(mean), vb = rbf(var);
  float den = rbf(sqrtf(rbf(vb + 1e-5f)));
  for (int i = threadIdx.x; i < D; i += 256) v[i] = rbf(rbf(v[i] - mb) / den);
  __syncthreads();
}
__device__ void gemv64(const u16* W, int ldw, int col0, int k0, int k1, const float* xs, float* part  , float* res) {
  int t = threadIdx.x, cc = t & 7, ro = t >> 3;
  float acc[8] = {0, 0, 0, 0, 0, 0, 0, 0};
#pragma unroll 8
  for (int r = k0 + ro; r < k1; r += 32) {
    v4u w = __builtin_nontemporal_load((const v4u*)(W + (size_t)r * ldw + col0 + cc * 8));
    float xv = xs[r - k0];
#pragma unroll
    for (int j = 0; j < 4; ++j) { acc[2 * j] += xv * bf2f(w[j] & 0xffffu); acc[2 * j + 1] += xv * __uint_as_float(w[j] & 0xffff0000u); }
  }
  for (int j = 0; j < 8; ++j) part[ro * 64 + cc * 8 + j] = acc[j];
  __syncthreads();
  if (t < 64) { float s = 0.f; for (int i = 0; i < 32; ++i) s += part[i * 64 + t]; res[t] = s; }
  __syncthreads();
}

__global__ void __launch_bounds__(256) k_mod(const u16* c, const u16* wmod, float* modp) {
  __shared__ float xs[256], part[32 * 64], res[64];
  int ks = blockIdx.y, k0 = ks * 256;
  { float cv = bf2f(c[k0 + threadIdx.x]); xs[threadIdx.x] = rbf(cv / (1.f + __expf(-cv))); }
  __syncthreads();
  gemv64(wmod, 6 * D, blockIdx.x * 64, k0, k0 + 256, xs, part, res);
  if (threadIdx.x < 64) modp[ks * 6144 + blockIdx.x * 64 + threadIdx.x] = res[threadIdx.x];
}
__device__ __forceinline__ float modv(const float* modp, int i) { return rbf(modp[i] + modp[6144 + i] + modp[2 * 6144 + i] + modp[3 * 6144 + i]); }
__global__ void __launch_bounds__(256) k_q(const u16* x, const float* modp, const u16* wq, float* qp) {
  __shared__ float xs[D], part[32 * 64], res[64], red[8];
  for (int i = threadIdx.x; i < D; i += 256) xs[i] = bf2f(x[i]);
  __syncthreads();
  ln_inplace(xs, red);
  for (int i = threadIdx.x; i < D; i += 256) xs[i] = rbf(rbf(xs[i] * rbf(1.f + modv(modp, D + i))) + modv(modp, i));
  __syncthreads();
  int ks = blockIdx.y, k0 = ks * 256;
  gemv64(wq, HQ * DH, blockIdx.x * 64, k0, k0 + 256, xs + k0, part, res);
  if (threadIdx.x < 64) qp[ks * 4096 + blockIdx.x * 64 + threadIdx.x] = res[threadIdx.x];
}
constexpr int NSPLIT = 128, KPB = SK / NSPLIT, KPW = KPB / 4;
__global__ void __launch_bounds__(256) k_attn(const float* qp, const u16* kc, const u16* vc, float* pm, float* pl, float* po) {
  __shared__ float sm[4][8], sl[4][8], so[4][8][128];
  int g = blockIdx.y, s = blockIdx.x, lane = threadIdx.x & 63, w = threadIdx.x >> 6, r = lane & 15, gq = lane >> 4;
  v4u qf[4];
  for (int cch = 0; cch < 4; ++cch) {
    unsigned e[8];
    for (int j = 0; j < 8; ++j) {
      int idx = (g * 8 + (r & 7)) * DH + cch * 32 + gq * 8 + j;
      float v = qp[idx] + qp[4096 + idx] + qp[8192 + idx] + qp[12288 + idx];
      e[j] = r < 8 ? f2bf(v) : 0u;
    }
    qf[cch] = v4u{e[0] | (e[1] << 16), e[2] | (e[3] << 16), e[4] | (e[5] << 16), e[6] | (e[7] << 16)};
  }
  size_t kbase = ((size_t)g * SK + (size_t)s * KPB + w * KPW) * DH;
  const u16* K = kc + kbase; const u16* V = vc + kbase;
  f32x4 sc[8];
  v4u kf[8][4];
#pragma unroll
  for (int b = 0; b < 8; ++b)
#pragma unroll
    for (int cch = 0; cch < 4; ++cch) kf[b][cch] = __builtin_nontemporal_load((const v4u*)(K + (size_t)(b * 16 + r) * DH + cch * 32 + gq * 8));
  __builtin_amdgcn_sched_barrier(0);
#pragma unroll
  for (int b = 0; b < 8; ++b) {
    f32x4 a = {0, 0, 0, 0};
#pragma unroll
    for (int cch = 0; cch < 4; ++cch) a = __builtin_amdgcn_mfma_f32_16x16x32_bf16(__builtin_bit_cast(bf16x8, kf[b][cch]), __builtin_bit_cast(bf16x8, qf[cch]), a, 0, 0, 0);
    sc[b] = a;
  }
  v4u vf[4][8];
#pragma unroll
  for (int t = 0; t < 4; ++t)
#pragma unroll
    for (int j = 0; j < 8; ++j) vf[t][j] = __builtin_nontemporal_load((const v4u*)(V + (size_t)(32 * t + (j >> 2) * 16 + 4 * gq + (j & 3)) * DH + r * 8));
  __builtin_amdgcn_sched_barrier(0);
  float m = -INFINITY;
#pragma unroll
  for (int b = 0; b < 8; ++b)
#pragma unroll
    for (int i = 0; i < 4; ++i) { float v = sc[b][i] * 0.08838834764831845f; sc[b][i] = v; m = fmaxf(m, v); }
  m = fmaxf(m, __shfl_xor(m, 16)); m = fmaxf(m, __shfl_xor(m, 32));
  float l = 0.f;
  unsigned pf[8][2];
#pragma unroll
  for (int b = 0; b < 8; ++b) {
    unsigned e[4];
#pragma unroll
    for (int i = 0; i < 4; ++i) { float p = __expf(sc[b][i] - m); l += p; e[i] = f2bf(p); }
    pf[b][0] = e[0] | (e[1] << 16); pf[b][1] = e[2] | (e[3] << 16);
  }
  l += __shfl_xor(l, 16); l += __shfl_xor(l, 32);
  f32x4 o[8];
#pragma unroll
  for (int jd = 0; jd < 8; ++jd) o[jd] = f32x4{0, 0, 0, 0};
#pragma unroll
  for (int t = 0; t < 4; ++t) {
    v4u pb = {pf[2 * t][0], pf[2 * t][1], pf[2 * t + 1][0], pf[2 * t + 1][1]};
#pragma unroll
    for (int jd = 0; jd < 8; ++jd) {
      v4u a;
#pragma unroll
      for (int u = 0; u < 4; ++u) {
        unsigned lo = vf[t][2 * u][jd >> 1], hi = vf[t][2 * u + 1][jd >> 1];
        a[u] = (jd & 1) ? __builtin_amdgcn_perm(hi, lo, 0x07060302u) : __builtin_amdgcn_perm(hi, lo, 0x05040100u);
      }
      o[jd] = __builtin_amdgcn_mfma_f32_16x16x32_bf16(__builtin_bit_cast(bf16x8, a), __builtin_bit_cast(bf16x8, pb), o[jd], 0, 0, 0);
    }
  }
  if (r < 8) {
#pragma unroll
    for (int jd = 0; jd < 8; ++jd)
#pragma unroll
      for (int i = 0; i < 4; ++i) so[w][r][8 * (4 * gq + i) + jd] = o[jd][i];
    if (gq == 0) { sm[w][r] = m; sl[w][r] = l; }
  }
  __syncthreads();
  for (int e = threadIdx.x; e < 8 * 128; e += 256) {
    int h = e >> 7, d = e & 127;
    float M = fmaxf(fmaxf(sm[0][h], sm[1][h]), fmaxf(sm[2][h], sm[3][h]));
    float acc = 0.f, L = 0.f;
    for (int ww = 0; ww < 4; ++ww) { float f = __expf(sm[ww][h] - M); acc += f * so[ww][h][d]; L += f * sl[ww][h]; }
    po[((size_t)(g * 8 + h) * NSPLIT + s) * 128 + d] = acc;
    if (d == 0) { pm[(g * 8 + h) * NSPLIT + s] = M; pl[(g * 8 + h) * NSPLIT + s] = L; }
  }
}
__global__ void __launch_bounds__(256) k_wo(const float* pm, const float* pl, const float* po, const u16* wo, float* ap) {
  __shared__ float xs[128], part[32 * 64], res[64], wts[NSPLIT], tmp[256], red[8];
  int h = blockIdx.y, t = threadIdx.x;
  float mv = t < NSPLIT ? pm[h * NSPLIT + t] : -INFINITY;
  float M = mv;
  for (int o = 32; o > 0; o >>= 1) M = fmaxf(M, __shfl_xor(M, o));
  if ((t & 63) == 0) red[t >> 6] = M;
  __syncthreads();
  M = fmaxf(fmaxf(red[0], red[1]), fmaxf(red[2], red[3]));
  float wv = t < NSPLIT ? __expf(mv - M) : 0.f;
  if (t < NSPLIT) wts[t] = wv;
  float L = block_sum(t < NSPLIT ? wv * pl[h * NSPLIT + t] : 0.f, red);
  int d = t & 127, half = t >> 7;
  float acc = 0.f;
  for (int s = half; s < NSPLIT; s += 2) acc += wts[s] * po[((size_t)h * NSPLIT + s) * 128 + d];
  tmp[t] = acc;
  __syncthreads();
  if (t < 128) xs[t] = rbf((tmp[t] + tmp[t + 128]) / L);
  __syncthreads();
  gemv64(wo, D, blockIdx.x * 64, h * 128, h * 128 + 128, xs, part, res);
  if (t < 64) ap[h * 1024 + blockIdx.x * 64 + t] = res[t];
}
__global__ void __launch_bounds__(256) k_w1(const u16* x, const float* modp, const float* ap, const u16* w1, float* hp, float* x2o) {
  __shared__ float xs[D], part[32 * 64], res[64], red[8];
  for (int i = threadIdx.x; i < D; i += 256) {
    float a = 0.f;
    for (int h = 0; h < 32; ++h) a += ap[h * 1024 + i];
    float x2 = rbf(bf2f(x[i]) + rbf(modv(modp, 2 * D + i) * rbf(a)));
    xs[i] = x2;
    if (blockIdx.x == 0 && blockIdx.y == 0) x2o[i] = x2;
  }
  __syncthreads();
  ln_inplace(xs, red);
  for (int i = threadIdx.x; i < D; i += 256) xs[i] = rbf(rbf(xs[i] * rbf(1.f + modv(modp, 4 * D + i))) + modv(modp, 3 * D + i));
  __syncthreads();
  ln_inplace(xs, red);
  int ks = blockIdx.y, k0 = ks * 256;
  gemv64(w1, F, blockIdx.x * 64, k0, k0 + 256, xs + k0, part, res);
  if (threadIdx.x < 64) hp[ks * F + blockIdx.x * 64 + threadIdx.x] = res[threadIdx.x];
}
__global__ void __launch_bounds__(256) k_w2(const float* hp, const u16* b1, const u16* w2, float* op) {
  __shared__ float xs[256], part[32 * 64], res[64];
  int ks = blockIdx.y, k0 = ks * 256, i = k0 + threadIdx.x;
  float t = rbf(rbf(hp[i] + hp[F + i] + hp[2 * F + i] + hp[3 * F + i]) + bf2f(b1[i]));
  float inner = 0.7978845608028654f * (t + 0.044715f * t * t * t);
  xs[threadIdx.x] = rbf(0.5f * t * (1.f + tanhf(inner)));
  __syncthreads();
  gemv64(w2, D, blockIdx.x * 64, k0, k0 + 256, xs, part, res);
  if (threadIdx.x < 64) op[ks * 1024 + blockIdx.x * 64 + threadIdx.x] = res[threadIdx.x];
}
__global__ void __launch_bounds__(256) k_out(const float* op, const u16* b2, const float* modp, const float* x2, u16* out) {
  int i = blockIdx.x * 256 + threadIdx.x;
  float a = 0.f;
  for (int s = 0; s < 14; ++s) a += op[s * 1024 + i];
  float u = rbf(rbf(a) + bf2f(b2[i]));
  out[i] = (u16)f2bf(x2[i] + rbf(modv(modp, 5 * D + i) * u));
}

extern "C" void kernel_launch(void* const* d_in, const int* in_sizes, int n_in, void* d_out, int out_size, void* d_ws, size_t ws_size, hipStream_t stream) {
  const u16* x = (const u16*)d_in[0]; const u16* kc = (const u16*)d_in[1]; const u16* vc = (const u16*)d_in[2];
  const u16* wq = (const u16*)d_in[3]; const u16* wo = (const u16*)d_in[4];
  const u16* w1 = (const u16*)d_in[6]; const u16* b1 = (const u16*)d_in[7]; const u16* w2 = (const u16*)d_in[8]; const u16* b2 = (const u16*)d_in[9];
  const u16* c = (const u16*)d_in[10]; const u16* wmod = (const u16*)d_in[11];
  float* ws = (float*)d_ws;
  float* modp = ws;
  float* qp = modp + 4 * 6144;
  float* pm = qp + 4 * 4096;
  float* pl = pm + 32 * NSPLIT;
  float* po = pl + 32 * NSPLIT;
  float* ap = po + 32 * NSPLIT * 128;
  float* hp = ap + 32 * 1024;
  float* x2 = hp + 4 * F;
  float* op = x2 + 1024;
  k_mod<<<dim3(96, 4), 256, 0, stream>>>(c, wmod, modp);
  k_q<<<dim3(64, 4), 256, 0, stream>>>(x, modp, wq, qp);
  k_attn<<<dim3(NSPLIT, 4), 256, 0, stream>>>(qp, kc, vc, pm, pl, po);
  k_wo<<<dim3(16, 32), 256, 0, stream>>>(pm, pl, po, wo, ap);
  k_w1<<<dim3(56, 4), 256, 0, stream>>>(x, modp, ap, w1, hp, x2);
  k_w2<<<dim3(16, 14), 256, 0, stream>>>(hp, b1, w2, op);
  k_out<<<dim3(4), 256, 0, stream>>>(op, b2, modp, x2, (u16*)d_out);
}
```

```cpp
#include <hip/hip_runtime.h>
#include <hip/hip_bf16.h>
#include <stdint.h>

typedef unsigned short u16;
typedef unsigned v4u __attribute__((ext_vector_type(4)));
typedef unsigned v2u __attribute__((ext_vector_type(2)));
typedef float f32x4 __attribute__((ext_vector_type(4)));
typedef float f32x2 __attribute__((ext_vector_type(2)));
typedef short bf16x8 __attribute__((ext_vector_type(8)));
typedef __bf16 bf16x2_t __attribute__((ext_vector_type(2)));
typedef __amdgpu_buffer_rsrc_t rsrc_t;

namespace {

constexpr int D = 1024, SK = 65536, DH = 128, FF = 3584;
constexpr int AUX_NT = 18, AUX_SC1 = 16;
constexpr unsigned SENT = 0xFFFFFFFFu;
#define WIN 4

constexpr int WS_EPOCH = 0;
constexpr int WS_G1 = 4096;
constexpr int WS_G2 = WS_G1 + 131072;
constexpr int WS_G3 = WS_G2 + 131072;
constexpr int WS_SENT = 512 * 1024;
constexpr int STR_A = 8192 + 128, STR_B = 16384 + 128, STR_D = 14336 + 128;
constexpr int WS_PA = WS_SENT;
constexpr int WS_PB = WS_PA + 128 * STR_A;
constexpr int WS_PD = WS_PB + 32 * STR_B;
constexpr int WS_ST = WS_PD + 32 * STR_D;
constexpr int WS_X2 = WS_ST + 16384;
constexpr int WS_SENT_END = WS_X2 + 2048;
static_assert(WS_SENT_END <= WS_SENT + 2 * 1024 * 1024, "sentinel region");
constexpr int WS_DIAG = WS_SENT + 2 * 1024 * 1024;
constexpr int WS_BYTES = WS_DIAG + 256 * 32 * 8;

constexpr int L_STASH = 0;
constexpr int L_RED = 65536;
constexpr int L_Q = L_RED + 32768;
constexpr int L_F1 = L_Q + 2048;
constexpr int L_F2 = L_F1 + 2048;
constexpr int L_F3 = L_F2 + 2048;
constexpr int L_H = L_F3 + 2048;
constexpr int L_ST = L_H + 2048;
constexpr int L_T1 = L_ST + 512;
constexpr int L_T2 = L_T1 + 1536;
constexpr int L_T3 = L_T2 + 1536;
constexpr int L_LN1 = L_T3 + 1536;
constexpr int L_LN2 = L_LN1 + 64;
constexpr int L_BYTES = L_LN2 + 64;

__device__ __forceinline__ float u2f(unsigned u) { return __builtin_bit_cast(float, u); }
__device__ __forceinline__ unsigned f2u(float f) { return __builtin_bit_cast(unsigned, f); }
__device__ __forceinline__ float bflo(unsigned p) { return u2f(p << 16); }
__device__ __forceinline__ float bfhi(unsigned p) { return u2f(p & 0xffff0000u); }
__device__ __forceinline__ unsigned pk2(float lo, float hi) {
  f32x2 v = {lo, hi};
  return __builtin_bit_cast(unsigned, __builtin_convertvector(v, bf16x2_t));
}
__device__ __forceinline__ float rbf(float f) { return bflo(pk2(f, f)); }
template <int CTRL> __device__ __forceinline__ unsigned dppu(unsigned x) { return (unsigned)__builtin_amdgcn_mov_dpp((int)x, CTRL, 0xf, 0xf, true); }
template <int CTRL> __device__ __forceinline__ float dppf(float x) { return u2f(dppu<CTRL>(f2u(x))); }
constexpr int XOR1 = 0xB1, XOR2 = 0x4E, XOR7 = 0x141, ROR8 = 0x128, ROR4 = 0x124, SHL1 = 0x101, SHL2 = 0x102, SHL4 = 0x104, SHL6 = 0x106;
__device__ __forceinline__ float sum_sw32(float a, float b) { auto r = __builtin_amdgcn_permlane32_swap(f2u(a), f2u(b), false, false); return u2f(r[0]) + u2f(r[1]); }
__device__ __forceinline__ float sum_sw16(float a, float b) { auto r = __builtin_amdgcn_permlane16_swap(f2u(a), f2u(b), false, false); return u2f(r[0]) + u2f(r[1]); }
__device__ __forceinline__ float max_sw32(float a) { auto r = __builtin_amdgcn_permlane32_swap(f2u(a), f2u(a), false, false); return fmaxf(u2f(r[0]), u2f(r[1])); }
__device__ __forceinline__ float max_sw16(float a) { auto r = __builtin_amdgcn_permlane16_swap(f2u(a), f2u(a), false, false); return fmaxf(u2f(r[0]), u2f(r[1])); }
__device__ __forceinline__ float wave_sum(float v) {
  v += dppf<XOR1>(v); v += dppf<XOR2>(v); v += dppf<XOR7>(v); v += dppf<ROR8>(v);
  v = sum_sw16(v, v); return sum_sw32(v, v);
}
__device__ __forceinline__ f32x4 mfma(v4u a, v4u b, f32x4 c) {
  return __builtin_amdgcn_mfma_f32_16x16x32_bf16(__builtin_bit_cast(bf16x8, a), __builtin_bit_cast(bf16x8, b), c, 0, 0, 0);
}
__device__ __forceinline__ rsrc_t mk(const void* p, int bytes) { return __builtin_amdgcn_make_buffer_rsrc((void*)p, 0, bytes, 0x00020000); }
template <int AUX> __device__ __forceinline__ v4u ld128(rsrc_t r, int vo, int so) { return __builtin_amdgcn_raw_buffer_load_b128(r, vo, so, AUX); }
template <int AUX> __device__ __forceinline__ v2u ld64(rsrc_t r, int vo, int so) { return __builtin_amdgcn_raw_buffer_load_b64(r, vo, so, AUX); }
template <int AUX> __device__ __forceinline__ unsigned ld32(rsrc_t r, int vo, int so) { return __builtin_amdgcn_raw_buffer_load_b32(r, vo, so, AUX); }
__device__ __forceinline__ void st128(v4u v, rsrc_t r, int vo, int so) { __builtin_amdgcn_raw_buffer_store_b128(v, r, vo, so, AUX_SC1); }
__device__ __forceinline__ void st64(v2u v, rsrc_t r, int vo, int so) { __builtin_amdgcn_raw_buffer_store_b64(v, r, vo, so, AUX_SC1); }
#define SB() __builtin_amdgcn_sched_barrier(0)
#define PACE(N) do { asm volatile("s_waitcnt vmcnt(%0)" ::"i"(N) : "memory"); SB(); } while (0)
#define TAGBAD(g) ((g[0].y != tag) | (g[0].w != tag) | (g[1].y != tag) | (g[1].w != tag) | (g[2].y != tag) | (g[2].w != tag) | (g[3].y != tag) | (g[3].w != tag))
#define CFENCE() asm volatile("" ::: "memory")
__device__ __forceinline__ void wg_barrier() {
  asm volatile("s_waitcnt lgkmcnt(0)" ::: "memory");
  __builtin_amdgcn_s_barrier();
  CFENCE();
}
__device__ __forceinline__ unsigned nosent(float v) { return min(f2u(v), 0xFFFFFFFEu); }
__device__ __forceinline__ bool has_sent(v4u v) { return max(max(v.x, v.y), max(v.z, v.w)) == SENT; }
__device__ __forceinline__ bool wave_any(bool p) { return __builtin_amdgcn_ballot_w64(p) != 0ull; }
__device__ __forceinline__ bool expired(unsigned long long t0) { return __builtin_amdgcn_s_memrealtime() - t0 > 5000000ull; }

__device__ __forceinline__ void fma8(float (&acc)[8], v4u w, float x) {
#pragma unroll
  for (int j = 0; j < 4; ++j) { acc[2 * j] = fmaf(x, bflo(w[j]), acc[2 * j]); acc[2 * j + 1] = fmaf(x, bfhi(w[j]), acc[2 * j + 1]); }
}
__device__ __forceinline__ float reduce8(const float (&a)[8], int lane) {
  float b0 = sum_sw32(a[0], a[4]), b1 = sum_sw32(a[1], a[5]), b2 = sum_sw32(a[2], a[6]), b3 = sum_sw32(a[3], a[7]);
  float c0 = sum_sw16(b0, b2), c1 = sum_sw16(b1, b3);
  const bool b = lane & 8;
  float keep = b ? c1 : c0, send = b ? c0 : c1;
  return keep + dppf<ROR8>(send);
}
__device__ __forceinline__ float reduce4(f32x4 v) {
  float a = sum_sw32(v[0], v[1]), b = sum_sw32(v[2], v[3]);
  float c = sum_sw16(a, b);
  return c + dppf<ROR8>(c);
}
__device__ __forceinline__ v4u tr8(const v4u* in, int jd) {
  v4u o;
#pragma unroll
  for (int u = 0; u < 4; ++u) o[u] = __builtin_amdgcn_perm(in[2 * u + 1][jd >> 1], in[2 * u][jd >> 1], (jd & 1) ? 0x07060302u : 0x05040100u);
  return o;
}
__device__ __forceinline__ v4u frag_from_lanes(float v) {
  unsigned p = pk2(v, dppf<SHL1>(v));
  return v4u{p, dppu<SHL2>(p), dppu<SHL4>(p), dppu<SHL6>(p)};
}
__device__ __forceinline__ float silu_bf(float x) { return rbf(x / (1.f + __expf(-x))); }

#define STAMP(i) do { } while (0)

__global__ void __launch_bounds__(512, 2)
fused_kernel(const u16* __restrict__ x, const u16* __restrict__ kc, const u16* __restrict__ vc, const u16* __restrict__ wq,
             const u16* __restrict__ wo, const u16* __restrict__ w1, const u16* __restrict__ b1, const u16* __restrict__ w2,
             const u16* __restrict__ b2, const u16* __restrict__ cnd, const u16* __restrict__ wmod, u16* __restrict__ out,
             unsigned char* ws) {
  __shared__ __attribute__((aligned(16))) unsigned char lds[L_BYTES];
  const int tid = threadIdx.x, lane = tid & 63, w = __builtin_amdgcn_readfirstlane(tid >> 6);
  const int cb = (blockIdx.x & 7) * 32 + (blockIdx.x >> 3);
  const int grp = cb >> 6, sp = cb & 63, ri = cb >> 1;
  const bool isX = (cb & 1) == 0;
  const int r16 = lane & 15, g4 = lane >> 4;
  const unsigned long long t0 = __builtin_amdgcn_s_memrealtime();
  const rsrc_t rW = mk(ws, WS_BYTES), rMod = mk(wmod, D * 6 * D * 2), rQ = mk(wq, D * 4096 * 2), rK = mk(kc, 4 * SK * DH * 2), rV = mk(vc, 4 * SK * DH * 2);
  const rsrc_t rC = mk(cnd, D * 2), rX = mk(x, D * 2);

  const int vo8 = (lane & 7) * 16;
  const int rg = lane >> 3;
  v4u f1w[2];
  {
    const int vo = (2 * rg) * 12288 + vo8, so = ((cb >> 5) * 128 + 16 * w) * 12288 + (cb & 31) * 128;
    f1w[0] = ld128<AUX_NT>(rMod, vo, so); f1w[1] = ld128<AUX_NT>(rMod, vo, so + 12288);
  }
  const unsigned c1 = ld32<0>(rC, rg * 4, ((cb >> 5) * 128 + 16 * w) * 2);
  const unsigned epoch = ld32<AUX_SC1>(rW, 0, WS_EPOCH);
  st128(v4u{SENT, SENT, SENT, SENT}, rW, tid * 16, WS_SENT + cb * 8192);
  const v4u xa = ld128<0>(rX, lane * 32, 0), xb = ld128<0>(rX, lane * 32, 16);
  const unsigned xh = ld32<0>(rX, r16 * 4, ((cb >> 6) * 256 + 32 * w) * 2);
  const v4u c3 = ld128<0>(rC, rg * 16, ((ri & 1) * 512 + 64 * w) * 2);
  SB();
  v4u f2w[4];
  {
    const int vo = (4 * rg) * 8192 + vo8, so = ((cb >> 6) * 256 + 32 * w) * 8192 + (cb & 63) * 128;
#pragma unroll
    for (int i = 0; i < 4; ++i) f2w[i] = ld128<AUX_NT>(rQ, vo, so + i * 8192);
  }
  SB();
  const unsigned tag = epoch + 1u;
  {
    float acc[8] = {0, 0, 0, 0, 0, 0, 0, 0};
    fma8(acc, f1w[0], silu_bf(bflo(c1))); fma8(acc, f1w[1], silu_bf(bfhi(c1)));
    ((float*)(lds + L_F1))[w * 64 + 8 * (lane & 7) + rg] = reduce8(acc, lane);
  }
  float ln_m, ln_den;
  {
    float v[16];
#pragma unroll
    for (int j = 0; j < 4; ++j) { v[2 * j] = bflo(xa[j]); v[2 * j + 1] = bfhi(xa[j]); v[8 + 2 * j] = bflo(xb[j]); v[9 + 2 * j] = bfhi(xb[j]); }
    float s = 0.f;
#pragma unroll
    for (int j = 0; j < 16; ++j) s += v[j];
    const float mean = wave_sum(s) * (1.f / D);
    float q = 0.f;
#pragma unroll
    for (int j = 0; j < 16; ++j) { const float d = v[j] - mean; q = fmaf(d, d, q); }
    const float var = wave_sum(q) * (1.f / D);
    ln_m = rbf(mean); ln_den = rbf(sqrtf(rbf(rbf(var) + 1e-5f)));
  }
  v4u rw[8];
  const rsrc_t rR = isX ? mk(wo, 4096 * D * 2) : rMod;
  const int r_vo = isX ? g4 * 8 * 2048 + r16 * 16 : (8 * rg) * 12288 + vo8;
  const int r_so = isX ? ri * 65536 + w * 256 : ((ri & 1) * 512 + 64 * w) * 12288 + 4096 + (ri >> 1) * 128;
  const int r_st = isX ? 2048 : 12288;
  PACE(4);
  rw[0] = ld128<AUX_NT>(rR, r_vo, r_so);
  wg_barrier();
  if (w == 0) {
    const float* f = (const float*)(lds + L_F1);
    float s = 0.f;
#pragma unroll
    for (int i = 0; i < 8; ++i) s += f[i * 64 + lane];
    st64(v2u{f2u(s), tag}, rW, lane * 8, WS_G1 + ((cb & 31) * 8 + (cb >> 5)) * 512);
  }
  STAMP(1);
#pragma unroll
  for (int i = 1; i < 8; ++i) { PACE(WIN - 1); rw[i] = ld128<AUX_NT>(rR, r_vo, r_so + i * r_st); }
  SB();

  const int k_vo = (4 * (lane & 3) + ((lane >> 2) & 1)) * 256 + ((lane >> 3) & 1) * 64 + g4 * 16;
  const int kv_so = cb * 262144 + w * 4096;
  v4u kv[32];
#define KLOADN(n, N) do { PACE(N); kv[n] = ld128<AUX_NT>(rK, k_vo + (((n) >> 1) & 1) * 512 + ((n) & 1) * 128, kv_so + ((n) >> 2) * 32768); SB(); } while (0)
#define VLOADN(n, N) do { PACE(N); vv[n] = ld128<AUX_NT>(rV, lane * 16 + ((n) & 3) * 1024, kv_so + ((n) >> 2) * 32768); SB(); } while (0)
#define KLOAD(n) KLOADN(n, WIN - 1)
#define VLOAD(n) VLOADN(n, WIN - 1)
  KLOAD(0); KLOAD(1); KLOAD(2); KLOAD(3);
  if (isX) {
#pragma unroll
    for (int jd = 0; jd < 8; ++jd) *(v4u*)(lds + L_STASH + (w * 8 + jd) * 1024 + lane * 16) = tr8(rw, jd);
  } else {
    float acc[8] = {0, 0, 0, 0, 0, 0, 0, 0};
#pragma unroll
    for (int i = 0; i < 8; ++i) fma8(acc, rw[i], silu_bf((i & 1) ? bfhi(c3[i >> 1]) : bflo(c3[i >> 1])));
    ((float*)(lds + L_F3))[w * 64 + 8 * (lane & 7) + rg] = reduce8(acc, lane);
  }
  SB();
  {
    const int e0 = (cb >> 6) * 256 + 32 * w;
    const int g_vo = ((g4 & 1) * 16 * 8 + (g4 >> 1) * 4) * 512 + r16 * 16;
    const int g_so = WS_G1 + (e0 >> 6) * 4096 + (e0 & 63) * 8;
    v4u g[4];
#pragma unroll
    for (int i = 0; i < 4; ++i) g[i] = ld128<AUX_SC1>(rW, g_vo + i * 512, g_so);
    SB();
    KLOADN(4, WIN + 3); KLOADN(5, WIN + 3); KLOADN(6, WIN + 3); KLOADN(7, WIN + 3);
    if (wave_any(TAGBAD(g))) {
      do {
        CFENCE();
        g[0] = ld128<AUX_SC1>(rW, g_vo, g_so); g[1] = ld128<AUX_SC1>(rW, g_vo + 512, g_so);
        g[2] = ld128<AUX_SC1>(rW, g_vo + 1024, g_so); g[3] = ld128<AUX_SC1>(rW, g_vo + 1536, g_so);
      } while (wave_any(TAGBAD(g)) && !expired(t0));
    }
    STAMP(2);
    float s0 = (u2f(g[0].x) + u2f(g[1].x)) + (u2f(g[2].x) + u2f(g[3].x)), s1 = (u2f(g[0].z) + u2f(g[1].z)) + (u2f(g[2].z) + u2f(g[3].z));
    s0 = rbf(sum_sw32(s0, s0)); s1 = rbf(sum_sw32(s1, s1));
    auto e0s = __builtin_amdgcn_permlane16_swap(f2u(s0), f2u(s0), false, false);
    auto e1s = __builtin_amdgcn_permlane16_swap(f2u(s1), f2u(s1), false, false);
    const float h0 = rbf(rbf(rbf(rbf(bflo(xh) - ln_m) / ln_den) * rbf(1.f + u2f(e0s[1]))) + u2f(e0s[0]));
    const float h1 = rbf(rbf(rbf(rbf(bfhi(xh) - ln_m) / ln_den) * rbf(1.f + u2f(e1s[1]))) + u2f(e1s[0]));
    if (lane < 16) *(f32x2*)(lds + L_H + w * 128 + r16 * 8) = f32x2{h0, h1};
    asm volatile("s_waitcnt lgkmcnt(0)" ::: "memory");
    const f32x4 hv = *(const f32x4*)(lds + L_H + w * 128 + rg * 16);
    float acc[8] = {0, 0, 0, 0, 0, 0, 0, 0};
#pragma unroll
    for (int i = 0; i < 4; ++i) fma8(acc, f2w[i], hv[i]);
    ((float*)(lds + L_F2))[w * 64 + 8 * (lane & 7) + rg] = reduce8(acc, lane);
  }
  KLOAD(8);
  wg_barrier();
  if (w == 0 || (w == 1 && !isX)) {
    const float* f = (const float*)(lds + (w == 0 ? L_F2 : L_F3));
    float s = 0.f;
#pragma unroll
    for (int i = 0; i < 8; ++i) s += f[i * 64 + lane];
    st64(v2u{f2u(s), tag}, rW, lane * 8, w == 0 ? WS_G2 + ((cb & 63) * 4 + (cb >> 6)) * 512 : WS_G3 + ri * 512);
  }
  STAMP(3);
  KLOAD(9); KLOAD(10); KLOAD(11); KLOAD(12); KLOAD(13); KLOAD(14); KLOAD(15);
  KLOAD(16); KLOAD(17); KLOAD(18); KLOAD(19); KLOAD(20); KLOAD(21); KLOAD(22); KLOAD(23);
  {
    const int g_vo = (tid >> 5) * 2048 + (tid & 31) * 16, g_so = WS_G2 + grp * 32768;
    v4u g[4];
#pragma unroll
    for (int i = 0; i < 4; ++i) g[i] = ld128<AUX_SC1>(rW, g_vo + i * 512, g_so);
    SB();
    KLOADN(24, WIN + 3); KLOADN(25, WIN + 3); KLOADN(26, WIN + 3); KLOADN(27, WIN + 3);
    if (wave_any(TAGBAD(g))) {
      do {
        CFENCE();
        g[0] = ld128<AUX_SC1>(rW, g_vo, g_so); g[1] = ld128<AUX_SC1>(rW, g_vo + 512, g_so);
        g[2] = ld128<AUX_SC1>(rW, g_vo + 1024, g_so); g[3] = ld128<AUX_SC1>(rW, g_vo + 1536, g_so);
      } while (wave_any(TAGBAD(g)) && !expired(t0));
    }
    const float s0 = (u2f(g[0].x) + u2f(g[1].x)) + (u2f(g[2].x) + u2f(g[3].x)), s1 = (u2f(g[0].z) + u2f(g[1].z)) + (u2f(g[2].z) + u2f(g[3].z));
    *(unsigned*)(lds + L_Q + tid * 4) = pk2(s0, s1);
  }
  KLOAD(28);
  wg_barrier();
  STAMP(4);
  if (cb == 0 && tid == 0) __builtin_amdgcn_raw_buffer_store_b32(tag, rW, 0, WS_EPOCH, AUX_SC1);
  KLOAD(29); KLOAD(30); KLOAD(31);
  const v4u q0 = *(const v4u*)(lds + L_Q + (r16 & 7) * 256 + (r16 >> 3) * 64 + g4 * 16);
  const v4u q1 = *(const v4u*)(lds + L_Q + (r16 & 7) * 256 + (r16 >> 3) * 64 + g4 * 16 + 128);

  v4u vv[32];
  float sc[32];
  float mx = -INFINITY;
#define QK(j) do { \
    f32x4 d0 = mfma(kv[4 * (j)], q0, f32x4{0, 0, 0, 0}); d0 = mfma(kv[4 * (j) + 1], q1, d0); \
    f32x4 d1 = mfma(kv[4 * (j) + 2], q0, f32x4{0, 0, 0, 0}); d1 = mfma(kv[4 * (j) + 3], q1, d1); \
    _Pragma("unroll") for (int i = 0; i < 4; ++i) { \
      auto r_ = __builtin_amdgcn_permlane32_swap(f2u(d0[i]), f2u(d1[i]), false, false); \
      const float s_ = rbf(rbf(u2f(r_[0]) + dppf<ROR8>(u2f(r_[1]))) * 0.08838834764831845f); \
      sc[4 * (j) + i] = s_; mx = fmaxf(mx, s_); } \
    SB(); } while (0)
  QK(0); VLOAD(0); QK(1); QK(2); VLOAD(1); QK(3); QK(4); VLOAD(2); QK(5); QK(6); VLOAD(3); QK(7);
  {
    mx = max_sw32(max_sw16(mx));
    float l = 0.f;
#pragma unroll
    for (int i = 0; i < 32; ++i) l += __expf(sc[i] - mx);
    l = sum_sw16(l, l); l = sum_sw32(l, l);
    if (lane < 8) *(f32x2*)(lds + L_ST + w * 64 + lane * 8) = f32x2{mx, l};
  }
  VLOAD(4);
  wg_barrier();
  if (w == 0) {
    const f32x2 ml = *(const f32x2*)(lds + L_ST + lane * 8);
    float M = fmaxf(ml[0], dppf<ROR8>(ml[0])); M = max_sw32(max_sw16(M));
    float L = ml[1] * __expf(ml[0] - M);
    L += dppf<ROR8>(L); L = sum_sw16(L, L); L = sum_sw32(L, L);
    if (lane < 8) st64(v2u{nosent(M), nosent(L)}, rW, lane * 8, WS_ST + cb * 64);
  }
  STAMP(5);
  VLOAD(5); VLOAD(6); VLOAD(7); VLOAD(8); VLOAD(9); VLOAD(10); VLOAD(11);
  const bool isRed = isX && (ri & 3) == 0;
  const int rj = ri >> 2;
  float m_a0, m_a1, m_b0, m_b1;
  {
    const int ea = isX ? 32 * rj + (lane & 30) : 2048 + 2 * tid, eb = isX ? 3072 + 32 * rj + (lane & 30) : 1024 + 2 * tid;
    const int va = (ea >> 6) * 1024 + (ea & 63) * 8, vb = (eb >> 6) * 1024 + (eb & 63) * 8;
    v4u g[4];
    g[0] = ld128<AUX_SC1>(rW, va, WS_G3); g[1] = ld128<AUX_SC1>(rW, va + 512, WS_G3);
    g[2] = ld128<AUX_SC1>(rW, vb, WS_G3); g[3] = ld128<AUX_SC1>(rW, vb + 512, WS_G3);
    SB();
    VLOADN(12, WIN + 3); VLOADN(13, WIN + 3); VLOADN(14, WIN + 3); VLOADN(15, WIN + 3);
    if (wave_any(TAGBAD(g))) {
      do {
        CFENCE();
        g[0] = ld128<AUX_SC1>(rW, va, WS_G3); g[1] = ld128<AUX_SC1>(rW, va + 512, WS_G3);
        g[2] = ld128<AUX_SC1>(rW, vb, WS_G3); g[3] = ld128<AUX_SC1>(rW, vb + 512, WS_G3);
      } while (wave_any(TAGBAD(g)) && !expired(t0));
    }
    m_a0 = rbf(u2f(g[0].x) + u2f(g[1].x)); m_a1 = rbf(u2f(g[0].z) + u2f(g[1].z));
    m_b0 = rbf(u2f(g[2].x) + u2f(g[3].x)); m_b1 = rbf(u2f(g[2].z) + u2f(g[3].z));
    if (isX) { if (lane & 1) { m_a0 = m_a1; m_b0 = m_b1; } }
    else { m_a0 = rbf(1.f + m_a0); m_a1 = rbf(1.f + m_a1); }
  }
  VLOAD(16); VLOAD(17); VLOAD(18); VLOAD(19);
  unsigned pf[16];
  {
    v2u g[8];
#pragma unroll
    for (int i = 0; i < 8; ++i) g[i] = ld64<AUX_SC1>(rW, lane * 8 + i * 512, WS_ST + grp * 4096);
    SB();
    VLOADN(20, WIN + 7); VLOADN(21, WIN + 7); VLOADN(22, WIN + 7); VLOADN(23, WIN + 7);
#define M2(i) max(g[i].x, g[i].y)
#define STBAD() (max(max(max(M2(0), M2(1)), max(M2(2), M2(3))), max(max(M2(4), M2(5)), max(M2(6), M2(7)))) == SENT)
#define STLD(i) g[i] = ld64<AUX_SC1>(rW, lane * 8 + (i) * 512, WS_ST + grp * 4096)
    if (wave_any(STBAD())) {
      do {
        CFENCE();
        STLD(0); STLD(1); STLD(2); STLD(3); STLD(4); STLD(5); STLD(6); STLD(7);
      } while (wave_any(STBAD()) && !expired(t0));
    }
    STAMP(6);
    float M = u2f(g[0].x);
#pragma unroll
    for (int i = 1; i < 8; ++i) M = fmaxf(M, u2f(g[i].x));
    M = fmaxf(M, dppf<ROR8>(M)); M = max_sw32(max_sw16(M));
    float L = 0.f;
#pragma unroll
    for (int i = 0; i < 8; ++i) L = fmaf(u2f(g[i].y), __expf(u2f(g[i].x) - M), L);
    L += dppf<ROR8>(L); L = sum_sw16(L, L); L = sum_sw32(L, L);
    const float rL = 1.f / L;
#pragma unroll
    for (int i = 0; i < 16; ++i) pf[i] = pk2(__expf(sc[2 * i] - M) * rL, __expf(sc[2 * i + 1] - M) * rL);
  }
  f32x4 acc[8];
#pragma unroll
  for (int jd = 0; jd < 8; ++jd) acc[jd] = f32x4{0, 0, 0, 0};
#define PV(t) do { \
    const v4u pa_ = {pf[4 * (t)], pf[4 * (t) + 1], pf[4 * (t) + 2], pf[4 * (t) + 3]}; \
    _Pragma("unroll") for (int jd = 0; jd < 8; ++jd) acc[jd] = mfma(pa_, tr8(&vv[8 * (t)], jd), acc[jd]); \
    SB(); } while (0)
  VLOAD(24); PV(0); VLOAD(25); VLOAD(26); PV(1); VLOAD(27); VLOAD(28); PV(2); VLOAD(29); VLOAD(30); VLOAD(31);
  PV(3);
  if (lane < 32) {
#pragma unroll
    for (int i = 0; i < 4; ++i) {
      float* p = (float*)(lds + L_RED + w * 4096 + (4 * g4 + i) * 512 + r16 * 16);
      *(f32x4*)p = f32x4{acc[0][i], acc[1][i], acc[2][i], acc[3][i]};
      *(f32x4*)(p + 64) = f32x4{acc[4][i], acc[5][i], acc[6][i], acc[7][i]};
    }
  }
  wg_barrier();
  if (w < 4) {
    const int head = tid >> 5, idx = tid & 31;
    const unsigned char* p = lds + L_RED + head * 512 + (idx & 1) * 256 + (idx >> 1) * 16;
    f32x4 s = *(const f32x4*)p;
#pragma unroll
    for (int i = 1; i < 8; ++i) s += *(const f32x4*)(p + i * 4096);
    st128(v4u{nosent(s[0]), nosent(s[1]), nosent(s[2]), nosent(s[3])}, rW, (head * 4 + (idx >> 3)) * STR_A + (idx & 7) * 16, WS_PA + grp * 32 * STR_A + sp * 128);
  }
  STAMP(7);
  const int ecomp = 4 * (lane & 7) + (g4 & 1) * 2 + (g4 >> 1);
  const int efrag = 8 * g4 + (lane & 7);
  const int out_vo = (r16 & 3) * 32 + (g4 >> 1) * 16;

  if (isX) {
    v4u wf[8];
#pragma unroll
    for (int jd = 0; jd < 8; ++jd) wf[jd] = *(const v4u*)(lds + L_STASH + (w * 8 + jd) * 1024 + lane * 16);
    unsigned xr = 0, b2r = 0;
    if (isRed) {
      xr = __builtin_amdgcn_raw_buffer_load_b16(rX, (lane & 31) * 2, rj * 64, 0);
      b2r = __builtin_amdgcn_raw_buffer_load_b16(mk(b2, D * 2), (lane & 31) * 2, rj * 64, 0);
    }
    v4u g;
    for (;;) {
      g = ld128<AUX_SC1>(rW, tid * 16, WS_PA + ri * STR_A);
      if (!wave_any(has_sent(g)) || expired(t0)) break;
      CFENCE();
    }
    const float part = reduce4(__builtin_bit_cast(f32x4, g));
    if (!(lane & 8)) ((float*)(lds + L_T1))[ecomp * 12 + w] = part;
    wg_barrier();
    STAMP(8);
    {
      const f32x4 a = *(const f32x4*)(lds + L_T1 + efrag * 48), b = *(const f32x4*)(lds + L_T1 + efrag * 48 + 16);
      const float o = ((a[0] + a[1]) + (a[2] + a[3])) + ((b[0] + b[1]) + (b[2] + b[3]));
      const v4u af = frag_from_lanes(o);
      f32x4 d[8];
#pragma unroll
      for (int jd = 0; jd < 8; ++jd) d[jd] = mfma(af, wf[jd], f32x4{0, 0, 0, 0});
      const bool hi = lane >= 32;
      const v4u o4 = {nosent(hi ? d[4][0] : d[0][0]), nosent(hi ? d[5][0] : d[1][0]), nosent(hi ? d[6][0] : d[2][0]), nosent(hi ? d[7][0] : d[3][0])};
      if (!(g4 & 1)) st128(o4, rW, (r16 >> 2) * STR_B + out_vo, WS_PB + 4 * w * STR_B + ri * 128);
    }
    STAMP(9);
    if (isRed) {
      v4u ga, gb;
      for (;;) {
        ga = ld128<AUX_SC1>(rW, tid * 16, WS_PB + rj * STR_B); gb = ld128<AUX_SC1>(rW, tid * 16, WS_PB + rj * STR_B + 8192);
        if (!wave_any(has_sent(ga) | has_sent(gb)) || expired(t0)) break;
        CFENCE();
      }
      const float p2 = reduce4(__builtin_bit_cast(f32x4, ga) + __builtin_bit_cast(f32x4, gb));
      if (!(lane & 8)) ((float*)(lds + L_T2))[ecomp * 12 + w] = p2;
      wg_barrier();
      STAMP(10);
      float x2 = 0.f;
      if (w == 0) {
        const f32x4 a = *(const f32x4*)(lds + L_T2 + (lane & 31) * 48), b = *(const f32x4*)(lds + L_T2 + (lane & 31) * 48 + 16);
        const float at = rbf(((a[0] + a[1]) + (a[2] + a[3])) + ((b[0] + b[1]) + (b[2] + b[3])));
        x2 = rbf(bflo(xr) + rbf(m_a0 * at));
        v4u f = frag_from_lanes(x2);
        f = v4u{min(f.x, 0xFFFFFFFEu), min(f.y, 0xFFFFFFFEu), min(f.z, 0xFFFFFFFEu), min(f.w, 0xFFFFFFFEu)};
        if (lane < 32 && !(lane & 7)) st128(f, rW, lane * 2, WS_X2 + rj * 64);
      }
      STAMP(11);
      gb = v4u{0, 0, 0, 0};
      for (;;) {
        ga = ld128<AUX_SC1>(rW, tid * 16, WS_PD + rj * STR_D);
        if (w < 6) gb = ld128<AUX_SC1>(rW, tid * 16, WS_PD + rj * STR_D + 8192);
        if (!wave_any(has_sent(ga) | has_sent(gb)) || expired(t0)) break;
        CFENCE();
      }
      const float p3 = reduce4(__builtin_bit_cast(f32x4, ga) + __builtin_bit_cast(f32x4, gb));
      if (!(lane & 8)) ((float*)(lds + L_T3))[ecomp * 12 + w] = p3;
      wg_barrier();
      STAMP(14);
      if (w == 0) {
        const f32x4 a = *(const f32x4*)(lds + L_T3 + (lane & 31) * 48), b = *(const f32x4*)(lds + L_T3 + (lane & 31) * 48 + 16);
        const float mo = rbf(((a[0] + a[1]) + (a[2] + a[3])) + ((b[0] + b[1]) + (b[2] + b[3])));
        const float u = rbf(mo + bflo(b2r));
        const float res = x2 + rbf(m_b0 * u);
        const v4u f = frag_from_lanes(res);
        if (lane < 32 && !(lane & 7)) *(v4u*)(out + rj * 32 + lane) = f;
      }
      STAMP(15);
    }
  } else if (ri < 112) {
    unsigned w1r[32];
    v4u w2r[8];
    {
      const rsrc_t r1 = mk(w1, D * FF * 2), r2 = mk(w2, FF * D * 2);
      const int vo1 = g4 * 8 * (FF * 2) + r16 * 4, so1 = 128 * w * (FF * 2) + ri * 64;
#pragma unroll
      for (int i = 0; i < 32; ++i) w1r[i] = ld32<0>(r1, vo1, so1 + ((i >> 3) * 32 + (i & 7)) * (FF * 2));
#pragma unroll
      for (int j = 0; j < 8; ++j) w2r[j] = ld128<AUX_NT>(r2, g4 * 8 * 2048 + r16 * 16, ri * 65536 + w * 256 + j * 2048);
    }
    const unsigned b1r = __builtin_amdgcn_raw_buffer_load_b16(mk(b1, FF * 2), efrag * 2, ri * 64, 0);
    SB();
    v4u f1[4][2], f2[8];
#pragma unroll
    for (int ks = 0; ks < 4; ++ks)
#pragma unroll
      for (int u = 0; u < 4; ++u) {
        f1[ks][0][u] = __builtin_amdgcn_perm(w1r[8 * ks + 2 * u + 1], w1r[8 * ks + 2 * u], 0x05040100u);
        f1[ks][1][u] = __builtin_amdgcn_perm(w1r[8 * ks + 2 * u + 1], w1r[8 * ks + 2 * u], 0x07060302u);
      }
#pragma unroll
    for (int jd = 0; jd < 8; ++jd) f2[jd] = tr8(w2r, jd);
    STAMP(16);
    unsigned xp;
    for (;;) {
      xp = ld32<AUX_SC1>(rW, tid * 4, WS_X2);
      if (!wave_any(xp == SENT) || expired(t0)) break;
      CFENCE();
    }
    STAMP(12);
    float v0 = bflo(xp), v1 = bfhi(xp);
#pragma unroll
    for (int pass = 0; pass < 2; ++pass) {
      const float piv = u2f(__builtin_amdgcn_readfirstlane(f2u(v0)));
      const float d0 = v0 - piv, d1 = v1 - piv;
      const float s1 = wave_sum(d0 + d1), s2 = wave_sum(fmaf(d0, d0, d1 * d1));
      unsigned char* lp = lds + (pass ? L_LN2 : L_LN1);
      if (lane == 0) *(f32x2*)(lp + w * 8) = f32x2{piv + s1 * (1.f / 128), s2 - s1 * s1 * (1.f / 128)};
      wg_barrier();
      f32x4 st[4];
#pragma unroll
      for (int i = 0; i < 4; ++i) st[i] = *(const f32x4*)(lp + i * 16);
      const float mean = (((st[0][0] + st[0][2]) + (st[1][0] + st[1][2])) + ((st[2][0] + st[2][2]) + (st[3][0] + st[3][2]))) * 0.125f;
      float m2 = ((st[0][1] + st[0][3]) + (st[1][1] + st[1][3])) + ((st[2][1] + st[2][3]) + (st[3][1] + st[3][3])), dv = 0.f;
#pragma unroll
      for (int i = 0; i < 4; ++i) { const float a = st[i][0] - mean, b = st[i][2] - mean; dv = fmaf(a, a, fmaf(b, b, dv)); }
      const float var = fmaxf(fmaf(dv, 128.f, m2), 0.f) * (1.f / D);
      const float mb = rbf(mean), den = rbf(sqrtf(rbf(rbf(var) + 1e-5f)));
      v0 = rbf(rbf(v0 - mb) / den); v1 = rbf(rbf(v1 - mb) / den);
      if (pass == 0) { v0 = rbf(rbf(v0 * m_a0) + m_b0); v1 = rbf(rbf(v1 * m_a1) + m_b1); }
    }
    *(unsigned*)(lds + L_H + tid * 4) = pk2(v0, v1);
    asm volatile("s_waitcnt lgkmcnt(0)" ::: "memory");
    f32x4 h0 = {0, 0, 0, 0}, h1 = {0, 0, 0, 0};
#pragma unroll
    for (int ks = 0; ks < 4; ++ks) {
      const v4u ya = *(const v4u*)(lds + L_H + w * 256 + ks * 64 + g4 * 16);
      h0 = mfma(ya, f1[ks][0], h0); h1 = mfma(ya, f1[ks][1], h1);
    }
    if (lane < 16) { ((float*)(lds + L_T1))[(2 * lane) * 12 + w] = h0[0]; ((float*)(lds + L_T1))[(2 * lane + 1) * 12 + w] = h1[0]; }
    wg_barrier();
    {
      const f32x4 a = *(const f32x4*)(lds + L_T1 + efrag * 48), b = *(const f32x4*)(lds + L_T1 + efrag * 48 + 16);
      const float t = rbf(rbf(((a[0] + a[1]) + (a[2] + a[3])) + ((b[0] + b[1]) + (b[2] + b[3]))) + bflo(b1r));
      const float inner = 0.7978845608028654f * (t + 0.044715f * t * t * t);
      const float th = 1.f - 2.f / (1.f + __expf(2.f * inner));
      const v4u af = frag_from_lanes(0.5f * t * (1.f + th));
      f32x4 d[8];
#pragma unroll
      for (int jd = 0; jd < 8; ++jd) d[jd] = mfma(af, f2[jd], f32x4{0, 0, 0, 0});
      const bool hi = lane >= 32;
      const v4u o4 = {nosent(hi ? d[4][0] : d[0][0]), nosent(hi ? d[5][0] : d[1][0]), nosent(hi ? d[6][0] : d[2][0]), nosent(hi ? d[7][0] : d[3][0])};
      if (!(g4 & 1)) st128(o4, rW, (r16 >> 2) * STR_D + out_vo, WS_PD + 4 * w * STR_D + ri * 128);
    }
    STAMP(13);
  }
}

}

extern "C" void kernel_launch(void* const* d_in, const int* in_sizes, int n_in, void* d_out, int out_size, void* d_ws, size_t ws_size,
                              hipStream_t stream) {
  (void)in_sizes; (void)n_in; (void)out_size; (void)ws_size;
  fused_kernel<<<dim3(256), dim3(512), 0, stream>>>(
      (const u16*)d_in[0], (const u16*)d_in[1], (const u16*)d_in[2], (const u16*)d_in[3], (const u16*)d_in[4], (const u16*)d_in[6],
      (const u16*)d_in[7], (const u16*)d_in[8], (const u16*)d_in[9], (const u16*)d_in[10], (const u16*)d_in[11], (u16*)d_out,
      (unsigned char*)d_ws);
}
```

```cpp
#include <hip/hip_runtime.h>
#include <hip/hip_bf16.h>
#include <stdint.h>

typedef unsigned short u16;
typedef unsigned v4u __attribute__((ext_vector_type(4)));
typedef unsigned v2u __attribute__((ext_vector_type(2)));
typedef float f32x4 __attribute__((ext_vector_type(4)));
typedef float f32x2 __attribute__((ext_vector_type(2)));
typedef short bf16x8 __attribute__((ext_vector_type(8)));
typedef __bf16 bf16x2_t __attribute__((ext_vector_type(2)));
typedef __amdgpu_buffer_rsrc_t rsrc_t;

namespace {

constexpr int D = 1024, SK = 65536, DH = 128, FF = 3584;
constexpr int AUX_NT = 2, AUX_SC1 = 16;
constexpr unsigned SENT = 0xFFFFFFFFu;
constexpr int CODE_BYTES = 21 * 1024;
#define WIN 4

constexpr int WS_EPOCH = 0;
constexpr int WS_G1 = 4096;
constexpr int WS_G2 = WS_G1 + 131072;
constexpr int WS_G3 = WS_G2 + 131072;
constexpr int WS_SENT = 512 * 1024;
constexpr int STR_A = 8192 + 128, STR_B = 16384 + 128, STR_D = 14336 + 128;
constexpr int WS_PA = WS_SENT;
constexpr int WS_PB = WS_PA + 128 * STR_A;
constexpr int WS_PD = WS_PB + 32 * STR_B;
constexpr int WS_ST = WS_PD + 32 * STR_D;
constexpr int WS_X2 = WS_ST + 16384;
constexpr int WS_SENT_END = WS_X2 + 2048;
static_assert(WS_SENT_END <= WS_SENT + 2 * 1024 * 1024, "sentinel region");
constexpr int WS_DIAG = WS_SENT + 2 * 1024 * 1024;
constexpr int WS_BYTES = WS_DIAG + 256 * 32 * 8;

constexpr int L_STASH = 0;
constexpr int L_RED = 65536;
constexpr int L_Q = L_RED + 32768;
constexpr int L_F1 = L_Q + 2048;
constexpr int L_F2 = L_F1 + 2048;
constexpr int L_F3 = L_F2 + 2048;
constexpr int L_H = L_F3 + 2048;
constexpr int L_ST = L_H + 2048;
constexpr int L_T1 = L_ST + 512;
constexpr int L_T2 = L_T1 + 1536;
constexpr int L_T3 = L_T2 + 1536;
constexpr int L_LN1 = L_T3 + 1536;
constexpr int L_LN2 = L_LN1 + 64;
constexpr int L_BYTES = L_LN2 + 64;

__device__ __forceinline__ float u2f(unsigned u) { return __builtin_bit_cast(float, u); }
__device__ __forceinline__ unsigned f2u(float f) { return __builtin_bit_cast(unsigned, f); }
__device__ __forceinline__ float bflo(unsigned p) { return u2f(p << 16); }
__device__ __forceinline__ float bfhi(unsigned p) { return u2f(p & 0xffff0000u); }
__device__ __forceinline__ unsigned pk2(float lo, float hi) {
  f32x2 v = {lo, hi};
  return __builtin_bit_cast(unsigned, __builtin_convertvector(v, bf16x2_t));
}
__device__ __forceinline__ float rbf(float f) { return bflo(pk2(f, f)); }
template <int CTRL> __device__ __forceinline__ unsigned dppu(unsigned x) { return (unsigned)__builtin_amdgcn_mov_dpp((int)x, CTRL, 0xf, 0xf, true); }
template <int CTRL> __device__ __forceinline__ float dppf(float x) { return u2f(dppu<CTRL>(f2u(x))); }
constexpr int XOR1 = 0xB1, XOR2 = 0x4E, XOR7 = 0x141, ROR8 = 0x128, ROR4 = 0x124, SHL1 = 0x101, SHL2 = 0x102, SHL4 = 0x104, SHL6 = 0x106;
__device__ __forceinline__ float sum_sw32(float a, float b) { auto r = __builtin_amdgcn_permlane32_swap(f2u(a), f2u(b), false, false); return u2f(r[0]) + u2f(r[1]); }
__device__ __forceinline__ float sum_sw16(float a, float b) { auto r = __builtin_amdgcn_permlane16_swap(f2u(a), f2u(b), false, false); return u2f(r[0]) + u2f(r[1]); }
__device__ __forceinline__ float max_sw32(float a) { auto r = __builtin_amdgcn_permlane32_swap(f2u(a), f2u(a), false, false); return fmaxf(u2f(r[0]), u2f(r[1])); }
__device__ __forceinline__ float max_sw16(float a) { auto r = __builtin_amdgcn_permlane16_swap(f2u(a), f2u(a), false, false); return fmaxf(u2f(r[0]), u2f(r[1])); }
__device__ __forceinline__ float wave_sum(float v) {
  v += dppf<XOR1>(v); v += dppf<XOR2>(v); v += dppf<XOR7>(v); v += dppf<ROR8>(v);
  v = sum_sw16(v, v); return sum_sw32(v, v);
}
__device__ __forceinline__ f32x4 mfma(v4u a, v4u b, f32x4 c) {
  return __builtin_amdgcn_mfma_f32_16x16x32_bf16(__builtin_bit_cast(bf16x8, a), __builtin_bit_cast(bf16x8, b), c, 0, 0, 0);
}
__device__ __forceinline__ rsrc_t mk(const void* p, int bytes) { return __builtin_amdgcn_make_buffer_rsrc((void*)p, 0, bytes, 0x00020000); }
template <int AUX> __device__ __forceinline__ v4u ld128(rsrc_t r, int vo, int so) { return __builtin_amdgcn_raw_buffer_load_b128(r, vo, so, AUX); }
template <int AUX> __device__ __forceinline__ v2u ld64(rsrc_t r, int vo, int so) { return __builtin_amdgcn_raw_buffer_load_b64(r, vo, so, AUX); }
template <int AUX> __device__ __forceinline__ unsigned ld32(rsrc_t r, int vo, int so) { return __builtin_amdgcn_raw_buffer_load_b32(r, vo, so, AUX); }
__device__ __forceinline__ void st128(v4u v, rsrc_t r, int vo, int so) { __builtin_amdgcn_raw_buffer_store_b128(v, r, vo, so, AUX_SC1); }
__device__ __forceinline__ void st64(v2u v, rsrc_t r, int vo, int so) { __builtin_amdgcn_raw_buffer_store_b64(v, r, vo, so, AUX_SC1); }
#define SB() __builtin_amdgcn_sched_barrier(0)
#define PACE(N) do { asm volatile("s_waitcnt vmcnt(%0)" ::"i"(N) : "memory"); SB(); } while (0)
#define TAGBAD(g) ((g[0].y != tag) | (g[0].w != tag) | (g[1].y != tag) | (g[1].w != tag) | (g[2].y != tag) | (g[2].w != tag) | (g[3].y != tag) | (g[3].w != tag))
#define CFENCE() asm volatile("" ::: "memory")
__device__ __forceinline__ void wg_barrier() {
  asm volatile("s_waitcnt lgkmcnt(0)" ::: "memory");
  __builtin_amdgcn_s_barrier();
  CFENCE();
}
__device__ __forceinline__ unsigned nosent(float v) { return min(f2u(v), 0xFFFFFFFEu); }
__device__ __forceinline__ bool has_sent(v4u v) { return max(max(v.x, v.y), max(v.z, v.w)) == SENT; }
__device__ __forceinline__ bool wave_any(bool p) { return __builtin_amdgcn_ballot_w64(p) != 0ull; }
__device__ __forceinline__ bool expired(unsigned long long t0) { return __builtin_amdgcn_s_memrealtime() - t0 > 5000000ull; }

__device__ __forceinline__ void fma8(float (&acc)[8], v4u w, float x) {
#pragma unroll
  for (int j = 0; j < 4; ++j) { acc[2 * j] = fmaf(x, bflo(w[j]), acc[2 * j]); acc[2 * j + 1] = fmaf(x, bfhi(w[j]), acc[2 * j + 1]); }
}
__device__ __forceinline__ float reduce8(const float (&a)[8], int lane) {
  float b0 = sum_sw32(a[0], a[4]), b1 = sum_sw32(a[1], a[5]), b2 = sum_sw32(a[2], a[6]), b3 = sum_sw32(a[3], a[7]);
  float c0 = sum_sw16(b0, b2), c1 = sum_sw16(b1, b3);
  const bool b = lane & 8;
  float keep = b ? c1 : c0, send = b ? c0 : c1;
  return keep + dppf<ROR8>(send);
}
__device__ __forceinline__ float reduce4(f32x4 v) {
  float a = sum_sw32(v[0], v[1]), b = sum_sw32(v[2], v[3]);
  float c = sum_sw16(a, b);
  return c + dppf<ROR8>(c);
}
__device__ __forceinline__ v4u tr8(const v4u* in, int jd) {
  v4u o;
#pragma unroll
  for (int u = 0; u < 4; ++u) o[u] = __builtin_amdgcn_perm(in[2 * u + 1][jd >> 1], in[2 * u][jd >> 1], (jd & 1) ? 0x07060302u : 0x05040100u);
  return o;
}
__device__ __forceinline__ v4u frag_from_lanes(float v) {
  unsigned p = pk2(v, dppf<SHL1>(v));
  return v4u{p, dppu<SHL2>(p), dppu<SHL4>(p), dppu<SHL6>(p)};
}
__device__ __forceinline__ float silu_bf(float x) { return rbf(x / (1.f + __expf(-x))); }

#define STAMP(i) do { } while (0)

__global__ void __launch_bounds__(512, 2)
fused_kernel(const u16* __restrict__ x, const u16* __restrict__ kc, const u16* __restrict__ vc, const u16* __restrict__ wq,
             const u16* __restrict__ wo, const u16* __restrict__ w1, const u16* __restrict__ b1, const u16* __restrict__ w2,
             const u16* __restrict__ b2, const u16* __restrict__ cnd, const u16* __restrict__ wmod, u16* __restrict__ out,
             unsigned char* ws) {
  __shared__ __attribute__((aligned(16))) unsigned char lds[L_BYTES];
  const int tid = threadIdx.x, lane = tid & 63, w = __builtin_amdgcn_readfirstlane(tid >> 6);
  const int cb = (blockIdx.x & 7) * 32 + (blockIdx.x >> 3);
  const int grp = cb >> 6, sp = cb & 63, ri = cb >> 1;
  const bool isX = (cb & 1) == 0;
  const int r16 = lane & 15, g4 = lane >> 4;
  const unsigned long long t0 = __builtin_amdgcn_s_memrealtime();
  const rsrc_t rW = mk(ws, WS_BYTES), rMod = mk(wmod, D * 6 * D * 2), rQ = mk(wq, D * 4096 * 2), rK = mk(kc, 4 * SK * DH * 2), rV = mk(vc, 4 * SK * DH * 2);
  const rsrc_t rC = mk(cnd, D * 2), rX = mk(x, D * 2);

  v4u pfc = {0, 0, 0, 0};
  {
    unsigned long long pc;
    asm volatile("s_getpc_b64 %0" : "=s"(pc));
    const rsrc_t rP = mk((const void*)(pc & ~255ull), CODE_BYTES);
    if (w == 7) pfc = ld128<0>(rP, lane * 16 + (blockIdx.x >> 3) * 1024, 0);
  }
  const int vo8 = (lane & 7) * 16;
  const int rg = lane >> 3;
  v4u f1w[2];
  {
    const int vo = (2 * rg) * 12288 + vo8, so = ((cb >> 5) * 128 + 16 * w) * 12288 + (cb & 31) * 128;
    f1w[0] = ld128<AUX_NT>(rMod, vo, so); f1w[1] = ld128<AUX_NT>(rMod, vo, so + 12288);
  }
  const unsigned c1 = ld32<0>(rC, rg * 4, ((cb >> 5) * 128 + 16 * w) * 2);
  const unsigned epoch = ld32<AUX_SC1>(rW, 0, WS_EPOCH);
  st128(v4u{SENT, SENT, SENT, SENT}, rW, tid * 16, WS_SENT + cb * 8192);
  const v4u xa = ld128<0>(rX, lane * 32, 0), xb = ld128<0>(rX, lane * 32, 16);
  const unsigned xh = ld32<0>(rX, r16 * 4, ((cb >> 6) * 256 + 32 * w) * 2);
  const v4u c3 = ld128<0>(rC, rg * 16, ((ri & 1) * 512 + 64 * w) * 2);
  SB();
  v4u f2w[4];
  {
    const int vo = (4 * rg) * 8192 + vo8, so = ((cb >> 6) * 256 + 32 * w) * 8192 + (cb & 63) * 128;
#pragma unroll
    for (int i = 0; i < 4; ++i) f2w[i] = ld128<AUX_NT>(rQ, vo, so + i * 8192);
  }
  SB();
  const unsigned tag = epoch + 1u;
  asm volatile("" ::"v"(pfc));
  {
    float acc[8] = {0, 0, 0, 0, 0, 0, 0, 0};
    fma8(acc, f1w[0], silu_bf(bflo(c1))); fma8(acc, f1w[1], silu_bf(bfhi(c1)));
    ((float*)(lds + L_F1))[w * 64 + 8 * (lane & 7) + rg] = reduce8(acc, lane);
  }
  float ln_m, ln_den;
  {
    float v[16];
#pragma unroll
    for (int j = 0; j < 4; ++j) { v[2 * j] = bflo(xa[j]); v[2 * j + 1] = bfhi(xa[j]); v[8 + 2 * j] = bflo(xb[j]); v[9 + 2 * j] = bfhi(xb[j]); }
    float s = 0.f;
#pragma unroll
    for (int j = 0; j < 16; ++j) s += v[j];
    const float mean = wave_sum(s) * (1.f / D);
    float q = 0.f;
#pragma unroll
    for (int j = 0; j < 16; ++j) { const float d = v[j] - mean; q = fmaf(d, d, q); }
    const float var = wave_sum(q) * (1.f / D);
    ln_m = rbf(mean); ln_den = rbf(sqrtf(rbf(rbf(var) + 1e-5f)));
  }
  v4u rw[8];
  const rsrc_t rR = isX ? mk(wo, 4096 * D * 2) : rMod;
  const int r_vo = isX ? g4 * 8 * 2048 + r16 * 16 : (8 * rg) * 12288 + vo8;
  const int r_so = isX ? ri * 65536 + w * 256 : ((ri & 1) * 512 + 64 * w) * 12288 + 4096 + (ri >> 1) * 128;
  const int r_st = isX ? 2048 : 12288;
  PACE(4);
  rw[0] = ld128<AUX_NT>(rR, r_vo, r_so);
  wg_barrier();
  if (w == 0) {
    const float* f = (const float*)(lds + L_F1);
    float s = 0.f;
#pragma unroll
    for (int i = 0; i < 8; ++i) s += f[i * 64 + lane];
    st64(v2u{f2u(s), tag}, rW, lane * 8, WS_G1 + ((cb & 31) * 8 + (cb >> 5)) * 512);
  }
  STAMP(1);
#pragma unroll
  for (int i = 1; i < 8; ++i) { PACE(WIN - 1); rw[i] = ld128<AUX_NT>(rR, r_vo, r_so + i * r_st); }
  SB();

  const int k_vo = (4 * (lane & 3) + ((lane >> 2) & 1)) * 256 + ((lane >> 3) & 1) * 64 + g4 * 16;
  const int kv_so = cb * 262144 + w * 4096;
  v4u kv[32];
#define KLOADN(n, N) do { PACE(N); kv[n] = ld128<AUX_NT>(rK, k_vo + (((n) >> 1) & 1) * 512 + ((n) & 1) * 128, kv_so + ((n) >> 2) * 32768); SB(); } while (0)
#define VLOADN(n, N) do { PACE(N); vv[n] = ld128<AUX_NT>(rV, lane * 16 + ((n) & 3) * 1024, kv_so + ((n) >> 2) * 32768); SB(); } while (0)
#define KLOAD(n) KLOADN(n, WIN - 1)
#define VLOAD(n) VLOADN(n, WIN - 1)
  KLOAD(0); KLOAD(1); KLOAD(2); KLOAD(3);
  if (isX) {
#pragma unroll
    for (int jd = 0; jd < 8; ++jd) *(v4u*)(lds + L_STASH + (w * 8 + jd) * 1024 + lane * 16) = tr8(rw, jd);
  } else {
    float acc[8] = {0, 0, 0, 0, 0, 0, 0, 0};
#pragma unroll
    for (int i = 0; i < 8; ++i) fma8(acc, rw[i], silu_bf((i & 1) ? bfhi(c3[i >> 1]) : bflo(c3[i >> 1])));
    ((float*)(lds + L_F3))[w * 64 + 8 * (lane & 7) + rg] = reduce8(acc, lane);
  }
  SB();
  {
    const int e0 = (cb >> 6) * 256 + 32 * w;
    const int g_vo = ((g4 & 1) * 16 * 8 + (g4 >> 1) * 4) * 512 + r16 * 16;
    const int g_so = WS_G1 + (e0 >> 6) * 4096 + (e0 & 63) * 8;
    v4u g[4];
#pragma unroll
    for (int i = 0; i < 4; ++i) g[i] = ld128<AUX_SC1>(rW, g_vo + i * 512, g_so);
    SB();
    KLOADN(4, WIN + 3); KLOADN(5, WIN + 3); KLOADN(6, WIN + 3); KLOADN(7, WIN + 3);
    if (wave_any(TAGBAD(g))) {
      do {
        CFENCE();
        g[0] = ld128<AUX_SC1>(rW, g_vo, g_so); g[1] = ld128<AUX_SC1>(rW, g_vo + 512, g_so);
        g[2] = ld128<AUX_SC1>(rW, g_vo + 1024, g_so); g[3] = ld128<AUX_SC1>(rW, g_vo + 1536, g_so);
      } while (wave_any(TAGBAD(g)) && !expired(t0));
    }
    STAMP(2);
    float s0 = (u2f(g[0].x) + u2f(g[1].x)) + (u2f(g[2].x) + u2f(g[3].x)), s1 = (u2f(g[0].z) + u2f(g[1].z)) + (u2f(g[2].z) + u2f(g[3].z));
    s0 = rbf(sum_sw32(s0, s0)); s1 = rbf(sum_sw32(s1, s1));
    auto e0s = __builtin_amdgcn_permlane16_swap(f2u(s0), f2u(s0), false, false);
    auto e1s = __builtin_amdgcn_permlane16_swap(f2u(s1), f2u(s1), false, false);
    const float h0 = rbf(rbf(rbf(rbf(bflo(xh) - ln_m) / ln_den) * rbf(1.f + u2f(e0s[1]))) + u2f(e0s[0]));
    const float h1 = rbf(rbf(rbf(rbf(bfhi(xh) - ln_m) / ln_den) * rbf(1.f + u2f(e1s[1]))) + u2f(e1s[0]));
    if (lane < 16) *(f32x2*)(lds + L_H + w * 128 + r16 * 8) = f32x2{h0, h1};
    asm volatile("s_waitcnt lgkmcnt(0)" ::: "memory");
    const f32x4 hv = *(const f32x4*)(lds + L_H + w * 128 + rg * 16);
    float acc[8] = {0, 0, 0, 0, 0, 0, 0, 0};
#pragma unroll
    for (int i = 0; i < 4; ++i) fma8(acc, f2w[i], hv[i]);
    ((float*)(lds + L_F2))[w * 64 + 8 * (lane & 7) + rg] = reduce8(acc, lane);
  }
  KLOAD(8);
  wg_barrier();
  if (w == 0 || (w == 1 && !isX)) {
    const float* f = (const float*)(lds + (w == 0 ? L_F2 : L_F3));
    float s = 0.f;
#pragma unroll
    for (int i = 0; i < 8; ++i) s += f[i * 64 + lane];
    st64(v2u{f2u(s), tag}, rW, lane * 8, w == 0 ? WS_G2 + ((cb & 63) * 4 + (cb >> 6)) * 512 : WS_G3 + ri * 512);
  }
  STAMP(3);
  KLOAD(9); KLOAD(10); KLOAD(11); KLOAD(12); KLOAD(13); KLOAD(14); KLOAD(15);
  KLOAD(16); KLOAD(17); KLOAD(18); KLOAD(19); KLOAD(20); KLOAD(21); KLOAD(22); KLOAD(23);
  {
    const int g_vo = (tid >> 5) * 2048 + (tid & 31) * 16, g_so = WS_G2 + grp * 32768;
    v4u g[4];
#pragma unroll
    for (int i = 0; i < 4; ++i) g[i] = ld128<AUX_SC1>(rW, g_vo + i * 512, g_so);
    SB();
    KLOADN(24, WIN + 3); KLOADN(25, WIN + 3); KLOADN(26, WIN + 3); KLOADN(27, WIN + 3);
    if (wave_any(TAGBAD(g))) {
      do {
        CFENCE();
        g[0] = ld128<AUX_SC1>(rW, g_vo, g_so); g[1] = ld128<AUX_SC1>(rW, g_vo + 512, g_so);
        g[2] = ld128<AUX_SC1>(rW, g_vo + 1024, g_so); g[3] = ld128<AUX_SC1>(rW, g_vo + 1536, g_so);
      } while (wave_any(TAGBAD(g)) && !expired(t0));
    }
    const float s0 = (u2f(g[0].x) + u2f(g[1].x)) + (u2f(g[2].x) + u2f(g[3].x)), s1 = (u2f(g[0].z) + u2f(g[1].z)) + (u2f(g[2].z) + u2f(g[3].z));
    *(unsigned*)(lds + L_Q + tid * 4) = pk2(s0, s1);
  }
  KLOAD(28);
  wg_barrier();
  STAMP(4);
  if (cb == 0 && tid == 0) __builtin_amdgcn_raw_buffer_store_b32(tag, rW, 0, WS_EPOCH, AUX_SC1);
  KLOAD(29); KLOAD(30); KLOAD(31);
  const v4u q0 = *(const v4u*)(lds + L_Q + (r16 & 7) * 256 + (r16 >> 3) * 64 + g4 * 16);
  const v4u q1 = *(const v4u*)(lds + L_Q + (r16 & 7) * 256 + (r16 >> 3) * 64 + g4 * 16 + 128);

  v4u vv[32];
  float sc[32];
  float mx = -INFINITY;
#define QK(j) do { \
    f32x4 d0 = mfma(kv[4 * (j)], q0, f32x4{0, 0, 0, 0}); d0 = mfma(kv[4 * (j) + 1], q1, d0); \
    f32x4 d1 = mfma(kv[4 * (j) + 2], q0, f32x4{0, 0, 0, 0}); d1 = mfma(kv[4 * (j) + 3], q1, d1); \
    _Pragma("unroll") for (int i = 0; i < 4; ++i) { \
      auto r_ = __builtin_amdgcn_permlane32_swap(f2u(d0[i]), f2u(d1[i]), false, false); \
      const float s_ = rbf(rbf(u2f(r_[0]) + dppf<ROR8>(u2f(r_[1]))) * 0.08838834764831845f); \
      sc[4 * (j) + i] = s_; mx = fmaxf(mx, s_); } \
    SB(); } while (0)
  QK(0); VLOAD(0); QK(1); QK(2); VLOAD(1); QK(3); QK(4); VLOAD(2); QK(5); QK(6); VLOAD(3); QK(7);
  {
    mx = max_sw32(max_sw16(mx));
    float l = 0.f;
#pragma unroll
    for (int i = 0; i < 32; ++i) l += __expf(sc[i] - mx);
    l = sum_sw16(l, l); l = sum_sw32(l, l);
    if (lane < 8) *(f32x2*)(lds + L_ST + w * 64 + lane * 8) = f32x2{mx, l};
  }
  VLOAD(4);
  wg_barrier();
  if (w == 0) {
    const f32x2 ml = *(const f32x2*)(lds + L_ST + lane * 8);
    float M = fmaxf(ml[0], dppf<ROR8>(ml[0])); M = max_sw32(max_sw16(M));
    float L = ml[1] * __expf(ml[0] - M);
    L += dppf<ROR8>(L); L = sum_sw16(L, L); L = sum_sw32(L, L);
    if (lane < 8) st64(v2u{nosent(M), nosent(L)}, rW, lane * 8, WS_ST + cb * 64);
  }
  STAMP(5);
  VLOAD(5); VLOAD(6); VLOAD(7); VLOAD(8); VLOAD(9); VLOAD(10); VLOAD(11);
  const bool isRed = isX && (ri & 3) == 0;
  const int rj = ri >> 2;
  float m_a0, m_a1, m_b0, m_b1;
  {
    const int ea = isX ? 32 * rj + (lane & 30) : 2048 + 2 * tid, eb = isX ? 3072 + 32 * rj + (lane & 30) : 1024 + 2 * tid;
    const int va = (ea >> 6) * 1024 + (ea & 63) * 8, vb = (eb >> 6) * 1024 + (eb & 63) * 8;
    v4u g[4];
    g[0] = ld128<AUX_SC1>(rW, va, WS_G3); g[1] = ld128<AUX_SC1>(rW, va + 512, WS_G3);
    g[2] = ld128<AUX_SC1>(rW, vb, WS_G3); g[3] = ld128<AUX_SC1>(rW, vb + 512, WS_G3);
    SB();
    VLOADN(12, WIN + 3); VLOADN(13, WIN + 3); VLOADN(14, WIN + 3); VLOADN(15, WIN + 3);
    if (wave_any(TAGBAD(g))) {
      do {
        CFENCE();
        g[0] = ld128<AUX_SC1>(rW, va, WS_G3); g[1] = ld128<AUX_SC1>(rW, va + 512, WS_G3);
        g[2] = ld128<AUX_SC1>(rW, vb, WS_G3); g[3] = ld128<AUX_SC1>(rW, vb + 512, WS_G3);
      } while (wave_any(TAGBAD(g)) && !expired(t0));
    }
    m_a0 = rbf(u2f(g[0].x) + u2f(g[1].x)); m_a1 = rbf(u2f(g[0].z) + u2f(g[1].z));
    m_b0 = rbf(u2f(g[2].x) + u2f(g[3].x)); m_b1 = rbf(u2f(g[2].z) + u2f(g[3].z));
    if (isX) { if (lane & 1) { m_a0 = m_a1; m_b0 = m_b1; } }
    else { m_a0 = rbf(1.f + m_a0); m_a1 = rbf(1.f + m_a1); }
  }
  VLOAD(16); VLOAD(17); VLOAD(18); VLOAD(19);
  unsigned pf[16];
  {
    v2u g[8];
#pragma unroll
    for (int i = 0; i < 8; ++i) g[i] = ld64<AUX_SC1>(rW, lane * 8 + i * 512, WS_ST + grp * 4096);
    SB();
    VLOADN(20, WIN + 7); VLOADN(21, WIN + 7); VLOADN(22, WIN + 7); VLOADN(23, WIN + 7);
#define M2(i) max(g[i].x, g[i].y)
#define STBAD() (max(max(max(M2(0), M2(1)), max(M2(2), M2(3))), max(max(M2(4), M2(5)), max(M2(6), M2(7)))) == SENT)
#define STLD(i) g[i] = ld64<AUX_SC1>(rW, lane * 8 + (i) * 512, WS_ST + grp * 4096)
    if (wave_any(STBAD())) {
      do {
        CFENCE();
        STLD(0); STLD(1); STLD(2); STLD(3); STLD(4); STLD(5); STLD(6); STLD(7);
      } while (wave_any(STBAD()) && !expired(t0));
    }
    STAMP(6);
    float M = u2f(g[0].x);
#pragma unroll
    for (int i = 1; i < 8; ++i) M = fmaxf(M, u2f(g[i].x));
    M = fmaxf(M, dppf<ROR8>(M)); M = max_sw32(max_sw16(M));
    float L = 0.f;
#pragma unroll
    for (int i = 0; i < 8; ++i) L = fmaf(u2f(g[i].y), __expf(u2f(g[i].x) - M), L);
    L += dppf<ROR8>(L); L = sum_sw16(L, L); L = sum_sw32(L, L);
    const float rL = 1.f / L;
#pragma unroll
    for (int i = 0; i < 16; ++i) pf[i] = pk2(__expf(sc[2 * i] - M) * rL, __expf(sc[2 * i + 1] - M) * rL);
  }
  f32x4 acc[8];
#pragma unroll
  for (int jd = 0; jd < 8; ++jd) acc[jd] = f32x4{0, 0, 0, 0};
#define PV(t) do { \
    const v4u pa_ = {pf[4 * (t)], pf[4 * (t) + 1], pf[4 * (t) + 2], pf[4 * (t) + 3]}; \
    _Pragma("unroll") for (int jd = 0; jd < 8; ++jd) acc[jd] = mfma(pa_, tr8(&vv[8 * (t)], jd), acc[jd]); \
    SB(); } while (0)
  VLOAD(24); PV(0); VLOAD(25); VLOAD(26); PV(1); VLOAD(27); VLOAD(28); PV(2); VLOAD(29); VLOAD(30); VLOAD(31);
  PV(3);
  if (lane < 32) {
#pragma unroll
    for (int i = 0; i < 4; ++i) {
      float* p = (float*)(lds + L_RED + w * 4096 + (4 * g4 + i) * 512 + r16 * 16);
      *(f32x4*)p = f32x4{acc[0][i], acc[1][i], acc[2][i], acc[3][i]};
      *(f32x4*)(p + 64) = f32x4{acc[4][i], acc[5][i], acc[6][i], acc[7][i]};
    }
  }
  wg_barrier();
  if (w < 4) {
    const int head = tid >> 5, idx = tid & 31;
    const unsigned char* p = lds + L_RED + head * 512 + (idx & 1) * 256 + (idx >> 1) * 16;
    f32x4 s = *(const f32x4*)p;
#pragma unroll
    for (int i = 1; i < 8; ++i) s += *(const f32x4*)(p + i * 4096);
    st128(v4u{nosent(s[0]), nosent(s[1]), nosent(s[2]), nosent(s[3])}, rW, (head * 4 + (idx >> 3)) * STR_A + (idx & 7) * 16, WS_PA + grp * 32 * STR_A + sp * 128);
  }
  STAMP(7);
  const int ecomp = 4 * (lane & 7) + (g4 & 1) * 2 + (g4 >> 1);
  const int efrag = 8 * g4 + (lane & 7);
  const int out_vo = (r16 & 3) * 32 + (g4 >> 1) * 16;

  if (isX) {
    v4u wf[8];
#pragma unroll
    for (int jd = 0; jd < 8; ++jd) wf[jd] = *(const v4u*)(lds + L_STASH + (w * 8 + jd) * 1024 + lane * 16);
    unsigned xr = 0, b2r = 0;
    if (isRed) {
      xr = __builtin_amdgcn_raw_buffer_load_b16(rX, (lane & 31) * 2, rj * 64, 0);
      b2r = __builtin_amdgcn_raw_buffer_load_b16(mk(b2, D * 2), (lane & 31) * 2, rj * 64, 0);
    }
    v4u g;
    for (;;) {
      g = ld128<AUX_SC1>(rW, tid * 16, WS_PA + ri * STR_A);
      if (!wave_any(has_sent(g)) || expired(t0)) break;
      CFENCE();
    }
    const float part = reduce4(__builtin_bit_cast(f32x4, g));
    if (!(lane & 8)) ((float*)(lds + L_T1))[ecomp * 12 + w] = part;
    wg_barrier();
    STAMP(8);
    {
      const f32x4 a = *(const f32x4*)(lds + L_T1 + efrag * 48), b = *(const f32x4*)(lds + L_T1 + efrag * 48 + 16);
      const float o = ((a[0] + a[1]) + (a[2] + a[3])) + ((b[0] + b[1]) + (b[2] + b[3]));
      const v4u af = frag_from_lanes(o);
      f32x4 d[8];
#pragma unroll
      for (int jd = 0; jd < 8; ++jd) d[jd] = mfma(af, wf[jd], f32x4{0, 0, 0, 0});
      const bool hi = lane >= 32;
      const v4u o4 = {nosent(hi ? d[4][0] : d[0][0]), nosent(hi ? d[5][0] : d[1][0]), nosent(hi ? d[6][0] : d[2][0]), nosent(hi ? d[7][0] : d[3][0])};
      if (!(g4 & 1)) st128(o4, rW, (r16 >> 2) * STR_B + out_vo, WS_PB + 4 * w * STR_B + ri * 128);
    }
    STAMP(9);
    if (isRed) {
      v4u ga, gb;
      for (;;) {
        ga = ld128<AUX_SC1>(rW, tid * 16, WS_PB + rj * STR_B); gb = ld128<AUX_SC1>(rW, tid * 16, WS_PB + rj * STR_B + 8192);
        if (!wave_any(has_sent(ga) | has_sent(gb)) || expired(t0)) break;
        CFENCE();
      }
      const float p2 = reduce4(__builtin_bit_cast(f32x4, ga) + __builtin_bit_cast(f32x4, gb));
      if (!(lane & 8)) ((float*)(lds + L_T2))[ecomp * 12 + w] = p2;
      wg_barrier();
      STAMP(10);
      float x2 = 0.f;
      if (w == 0) {
        const f32x4 a = *(const f32x4*)(lds + L_T2 + (lane & 31) * 48), b = *(const f32x4*)(lds + L_T2 + (lane & 31) * 48 + 16);
        const float at = rbf(((a[0] + a[1]) + (a[2] + a[3])) + ((b[0] + b[1]) + (b[2] + b[3])));
        x2 = rbf(bflo(xr) + rbf(m_a0 * at));
        v4u f = frag_from_lanes(x2);
        f = v4u{min(f.x, 0xFFFFFFFEu), min(f.y, 0xFFFFFFFEu), min(f.z, 0xFFFFFFFEu), min(f.w, 0xFFFFFFFEu)};
        if (lane < 32 && !(lane & 7)) st128(f, rW, lane * 2, WS_X2 + rj * 64);
      }
      STAMP(11);
      gb = v4u{0, 0, 0, 0};
      for (;;) {
        ga = ld128<AUX_SC1>(rW, tid * 16, WS_PD + rj * STR_D);
        if (w < 6) gb = ld128<AUX_SC1>(rW, tid * 16, WS_PD + rj * STR_D + 8192);
        if (!wave_any(has_sent(ga) | has_sent(gb)) || expired(t0)) break;
        CFENCE();
      }
      const float p3 = reduce4(__builtin_bit_cast(f32x4, ga) + __builtin_bit_cast(f32x4, gb));
      if (!(lane & 8)) ((float*)(lds + L_T3))[ecomp * 12 + w] = p3;
      wg_barrier();
      STAMP(14);
      if (w == 0) {
        const f32x4 a = *(const f32x4*)(lds + L_T3 + (lane & 31) * 48), b = *(const f32x4*)(lds + L_T3 + (lane & 31) * 48 + 16);
        const float mo = rbf(((a[0] + a[1]) + (a[2] + a[3])) + ((b[0] + b[1]) + (b[2] + b[3])));
        const float u = rbf(mo + bflo(b2r));
        const float res = x2 + rbf(m_b0 * u);
        const v4u f = frag_from_lanes(res);
        if (lane < 32 && !(lane & 7)) *(v4u*)(out + rj * 32 + lane) = f;
      }
      STAMP(15);
    }
  } else if (ri < 112) {
    unsigned w1r[32];
    v4u w2r[8];
    {
      const rsrc_t r1 = mk(w1, D * FF * 2), r2 = mk(w2, FF * D * 2);
      const int vo1 = g4 * 8 * (FF * 2) + r16 * 4, so1 = 128 * w * (FF * 2) + ri * 64;
#pragma unroll
      for (int i = 0; i < 32; ++i) w1r[i] = ld32<0>(r1, vo1, so1 + ((i >> 3) * 32 + (i & 7)) * (FF * 2));
#pragma unroll
      for (int j = 0; j < 8; ++j) w2r[j] = ld128<AUX_NT>(r2, g4 * 8 * 2048 + r16 * 16, ri * 65536 + w * 256 + j * 2048);
    }
    const unsigned b1r = __builtin_amdgcn_raw_buffer_load_b16(mk(b1, FF * 2), efrag * 2, ri * 64, 0);
    SB();
    v4u f1[4][2], f2[8];
#pragma unroll
    for (int ks = 0; ks < 4; ++ks)
#pragma unroll
      for (int u = 0; u < 4; ++u) {
        f1[ks][0][u] = __builtin_amdgcn_perm(w1r[8 * ks + 2 * u + 1], w1r[8 * ks + 2 * u], 0x05040100u);
        f1[ks][1][u] = __builtin_amdgcn_perm(w1r[8 * ks + 2 * u + 1], w1r[8 * ks + 2 * u], 0x07060302u);
      }
#pragma unroll
    for (int jd = 0; jd < 8; ++jd) f2[jd] = tr8(w2r, jd);
    STAMP(16);
    unsigned xp;
    for (;;) {
      xp = ld32<AUX_SC1>(rW, tid * 4, WS_X2);
      if (!wave_any(xp == SENT) || expired(t0)) break;
      CFENCE();
    }
    STAMP(12);
    float v0 = bflo(xp), v1 = bfhi(xp);
#pragma unroll
    for (int pass = 0; pass < 2; ++pass) {
      const float piv = u2f(__builtin_amdgcn_readfirstlane(f2u(v0)));
      const float d0 = v0 - piv, d1 = v1 - piv;
      const float s1 = wave_sum(d0 + d1), s2 = wave_sum(fmaf(d0, d0, d1 * d1));
      unsigned char* lp = lds + (pass ? L_LN2 : L_LN1);
      if (lane == 0) *(f32x2*)(lp + w * 8) = f32x2{piv + s1 * (1.f / 128), s2 - s1 * s1 * (1.f / 128)};
      wg_barrier();
      f32x4 st[4];
#pragma unroll
      for (int i = 0; i < 4; ++i) st[i] = *(const f32x4*)(lp + i * 16);
      const float mean = (((st[0][0] + st[0][2]) + (st[1][0] + st[1][2])) + ((st[2][0] + st[2][2]) + (st[3][0] + st[3][2]))) * 0.125f;
      float m2 = ((st[0][1] + st[0][3]) + (st[1][1] + st[1][3])) + ((st[2][1] + st[2][3]) + (st[3][1] + st[3][3])), dv = 0.f;
#pragma unroll
      for (int i = 0; i < 4; ++i) { const float a = st[i][0] - mean, b = st[i][2] - mean; dv = fmaf(a, a, fmaf(b, b, dv)); }
      const float var = fmaxf(fmaf(dv, 128.f, m2), 0.f) * (1.f / D);
      const float mb = rbf(mean), den = rbf(sqrtf(rbf(rbf(var) + 1e-5f)));
      v0 = rbf(rbf(v0 - mb) / den); v1 = rbf(rbf(v1 - mb) / den);
      if (pass == 0) { v0 = rbf(rbf(v0 * m_a0) + m_b0); v1 = rbf(rbf(v1 * m_a1) + m_b1); }
    }
    *(unsigned*)(lds + L_H + tid * 4) = pk2(v0, v1);
    asm volatile("s_waitcnt lgkmcnt(0)" ::: "memory");
    f32x4 h0 = {0, 0, 0, 0}, h1 = {0, 0, 0, 0};
#pragma unroll
    for (int ks = 0; ks < 4; ++ks) {
      const v4u ya = *(const v4u*)(lds + L_H + w * 256 + ks * 64 + g4 * 16);
      h0 = mfma(ya, f1[ks][0], h0); h1 = mfma(ya, f1[ks][1], h1);
    }
    if (lane < 16) { ((float*)(lds + L_T1))[(2 * lane) * 12 + w] = h0[0]; ((float*)(lds + L_T1))[(2 * lane + 1) * 12 + w] = h1[0]; }
    wg_barrier();
    {
      const f32x4 a = *(const f32x4*)(lds + L_T1 + efrag * 48), b = *(const f32x4*)(lds + L_T1 + efrag * 48 + 16);
      const float t = rbf(rbf(((a[0] + a[1]) + (a[2] + a[3])) + ((b[0] + b[1]) + (b[2] + b[3]))) + bflo(b1r));
      const float inner = 0.7978845608028654f * (t + 0.044715f * t * t * t);
      const float th = 1.f - 2.f / (1.f + __expf(2.f * inner));
      const v4u af = frag_from_lanes(0.5f * t * (1.f + th));
      f32x4 d[8];
#pragma unroll
      for (int jd = 0; jd < 8; ++jd) d[jd] = mfma(af, f2[jd], f32x4{0, 0, 0, 0});
      const bool hi = lane >= 32;
      const v4u o4 = {nosent(hi ? d[4][0] : d[0][0]), nosent(hi ? d[5][0] : d[1][0]), nosent(hi ? d[6][0] : d[2][0]), nosent(hi ? d[7][0] : d[3][0])};
      if (!(g4 & 1)) st128(o4, rW, (r16 >> 2) * STR_D + out_vo, WS_PD + 4 * w * STR_D + ri * 128);
    }
    STAMP(13);
  }
}

}

extern "C" void kernel_launch(void* const* d_in, const int* in_sizes, int n_in, void* d_out, int out_size, void* d_ws, size_t ws_size,
                              hipStream_t stream) {
  (void)in_sizes; (void)n_in; (void)out_size; (void)ws_size;
  fused_kernel<<<dim3(256), dim3(512), 0, stream>>>(
      (const u16*)d_in[0], (const u16*)d_in[1], (const u16*)d_in[2], (const u16*)d_in[3], (const u16*)d_in[4], (const u16*)d_in[6],
      (const u16*)d_in[7], (const u16*)d_in[8], (const u16*)d_in[9], (const u16*)d_in[10], (const u16*)d_in[11], (u16*)d_out,
      (unsigned char*)d_ws);
}
```

```cpp
#define DIAGH 0
#include <hip/hip_runtime.h>
#include <hip/hip_bf16.h>
#include <stdint.h>

typedef unsigned short u16;
typedef unsigned v4u __attribute__((ext_vector_type(4)));
typedef unsigned v2u __attribute__((ext_vector_type(2)));
typedef float f32x4 __attribute__((ext_vector_type(4)));
typedef float f32x2 __attribute__((ext_vector_type(2)));
typedef short bf16x8 __attribute__((ext_vector_type(8)));
typedef __bf16 bf16x2_t __attribute__((ext_vector_type(2)));
typedef __amdgpu_buffer_rsrc_t rsrc_t;

namespace {

constexpr int D = 1024, SK = 65536, DH = 128, FF = 3584;
constexpr int AUX_NT = 2, AUX_SC1 = 16;
constexpr unsigned SENT = 0xFFFFFFFFu;
constexpr int CODE_BYTES = 21 * 1024;
#define WIN 4

constexpr int WS_EPOCH = 0;
constexpr int WS_G1 = 4096;
constexpr int WS_G2 = WS_G1 + 131072;
constexpr int WS_G3 = WS_G2 + 131072;
constexpr int WS_SENT = 512 * 1024;
constexpr int STR_A = 8192 + 128, STR_B = 16384 + 128, STR_D = 14336 + 128;
constexpr int WS_PA = WS_SENT;
constexpr int WS_PB = WS_PA + 128 * STR_A;
constexpr int WS_PD = WS_PB + 32 * STR_B;
constexpr int WS_ST = WS_PD + 32 * STR_D;
constexpr int WS_X2 = WS_ST + 16384;
constexpr int WS_SENT_END = WS_X2 + 2048;
static_assert(WS_SENT_END <= WS_SENT + 2 * 1024 * 1024, "sentinel region");
constexpr int WS_DIAG = WS_SENT + 2 * 1024 * 1024;
constexpr int WS_BYTES = WS_DIAG + 256 * 32 * 8;

constexpr int L_STASH = 0;
constexpr int L_RED = 65536;
constexpr int L_Q = L_RED + 32768;
constexpr int L_F1 = L_Q + 2048;
constexpr int L_F2 = L_F1 + 2048;
constexpr int L_F3 = L_F2 + 2048;
constexpr int L_H = L_F3 + 2048;
constexpr int L_ST = L_H + 2048;
constexpr int L_T1 = L_ST + 512;
constexpr int L_T2 = L_T1 + 1536;
constexpr int L_T3 = L_T2 + 1536;
constexpr int L_LN1 = L_T3 + 1536;
constexpr int L_LN2 = L_LN1 + 64;
constexpr int L_BYTES = L_LN2 + 64;

__device__ __forceinline__ float u2f(unsigned u) { return __builtin_bit_cast(float, u); }
__device__ __forceinline__ unsigned f2u(float f) { return __builtin_bit_cast(unsigned, f); }
__device__ __forceinline__ float bflo(unsigned p) { return u2f(p << 16); }
__device__ __forceinline__ float bfhi(unsigned p) { return u2f(p & 0xffff0000u); }
__device__ __forceinline__ unsigned pk2(float lo, float hi) {
  f32x2 v = {lo, hi};
  return __builtin_bit_cast(unsigned, __builtin_convertvector(v, bf16x2_t));
}
__device__ __forceinline__ float rbf(float f) { return bflo(pk2(f, f)); }
template <int CTRL> __device__ __forceinline__ unsigned dppu(unsigned x) { return (unsigned)__builtin_amdgcn_mov_dpp((int)x, CTRL, 0xf, 0xf, true); }
template <int CTRL> __device__ __forceinline__ float dppf(float x) { return u2f(dppu<CTRL>(f2u(x))); }
constexpr int XOR1 = 0xB1, XOR2 = 0x4E, XOR7 = 0x141, ROR8 = 0x128, ROR4 = 0x124, SHL1 = 0x101, SHL2 = 0x102, SHL4 = 0x104, SHL6 = 0x106;
__device__ __forceinline__ float sum_sw32(float a, float b) { auto r = __builtin_amdgcn_permlane32_swap(f2u(a), f2u(b), false, false); return u2f(r[0]) + u2f(r[1]); }
__device__ __forceinline__ float sum_sw16(float a, float b) { auto r = __builtin_amdgcn_permlane16_swap(f2u(a), f2u(b), false, false); return u2f(r[0]) + u2f(r[1]); }
__device__ __forceinline__ float max_sw32(float a) { auto r = __builtin_amdgcn_permlane32_swap(f2u(a), f2u(a), false, false); return fmaxf(u2f(r[0]), u2f(r[1])); }
__device__ __forceinline__ float max_sw16(float a) { auto r = __builtin_amdgcn_permlane16_swap(f2u(a), f2u(a), false, false); return fmaxf(u2f(r[0]), u2f(r[1])); }
__device__ __forceinline__ float wave_sum(float v) {
  v += dppf<XOR1>(v); v += dppf<XOR2>(v); v += dppf<XOR7>(v); v += dppf<ROR8>(v);
  v = sum_sw16(v, v); return sum_sw32(v, v);
}
__device__ __forceinline__ f32x4 mfma(v4u a, v4u b, f32x4 c) {
  return __builtin_amdgcn_mfma_f32_16x16x32_bf16(__builtin_bit_cast(bf16x8, a), __builtin_bit_cast(bf16x8, b), c, 0, 0, 0);
}
__device__ __forceinline__ rsrc_t mk(const void* p, int bytes) { return __builtin_amdgcn_make_buffer_rsrc((void*)p, 0, bytes, 0x00020000); }
template <int AUX> __device__ __forceinline__ v4u ld128(rsrc_t r, int vo, int so) { return __builtin_amdgcn_raw_buffer_load_b128(r, vo, so, AUX); }
template <int AUX> __device__ __forceinline__ v2u ld64(rsrc_t r, int vo, int so) { return __builtin_amdgcn_raw_buffer_load_b64(r, vo, so, AUX); }
template <int AUX> __device__ __forceinline__ unsigned ld32(rsrc_t r, int vo, int so) { return __builtin_amdgcn_raw_buffer_load_b32(r, vo, so, AUX); }
__device__ __forceinline__ void st128(v4u v, rsrc_t r, int vo, int so) { __builtin_amdgcn_raw_buffer_store_b128(v, r, vo, so, AUX_SC1); }
__device__ __forceinline__ void st64(v2u v, rsrc_t r, int vo, int so) { __builtin_amdgcn_raw_buffer_store_b64(v, r, vo, so, AUX_SC1); }
#define SB() __builtin_amdgcn_sched_barrier(0)
#define PACE(N) do { asm volatile("s_waitcnt vmcnt(%0)" ::"i"(N) : "memory"); SB(); } while (0)
#define TAGBAD(g) ((g[0].y != tag) | (g[0].w != tag) | (g[1].y != tag) | (g[1].w != tag) | (g[2].y != tag) | (g[2].w != tag) | (g[3].y != tag) | (g[3].w != tag))
#define CFENCE() asm volatile("" ::: "memory")
__device__ __forceinline__ void wg_barrier() {
  asm volatile("s_waitcnt lgkmcnt(0)" ::: "memory");
  __builtin_amdgcn_s_barrier();
  CFENCE();
}
__device__ __forceinline__ unsigned nosent(float v) { return min(f2u(v), 0xFFFFFFFEu); }
__device__ __forceinline__ bool has_sent(v4u v) { return max(max(v.x, v.y), max(v.z, v.w)) == SENT; }
__device__ __forceinline__ bool wave_any(bool p) { return __builtin_amdgcn_ballot_w64(p) != 0ull; }
__device__ __forceinline__ bool expired(unsigned long long t0) { return __builtin_amdgcn_s_memrealtime() - t0 > 5000000ull; }

__device__ __forceinline__ void fma8(float (&acc)[8], v4u w, float x) {
#pragma unroll
  for (int j = 0; j < 4; ++j) { acc[2 * j] = fmaf(x, bflo(w[j]), acc[2 * j]); acc[2 * j + 1] = fmaf(x, bfhi(w[j]), acc[2 * j + 1]); }
}
__device__ __forceinline__ float reduce8(const float (&a)[8], int lane) {
  float b0 = sum_sw32(a[0], a[4]), b1 = sum_sw32(a[1], a[5]), b2 = sum_sw32(a[2], a[6]), b3 = sum_sw32(a[3], a[7]);
  float c0 = sum_sw16(b0, b2), c1 = sum_sw16(b1, b3);
  const bool b = lane & 8;
  float keep = b ? c1 : c0, send = b ? c0 : c1;
  return keep + dppf<ROR8>(send);
}
__device__ __forceinline__ float reduce4(f32x4 v) {
  float a = sum_sw32(v[0], v[1]), b = sum_sw32(v[2], v[3]);
  float c = sum_sw16(a, b);
  return c + dppf<ROR8>(c);
}
__device__ __forceinline__ v4u tr8(const v4u* in, int jd) {
  v4u o;
#pragma unroll
  for (int u = 0; u < 4; ++u) o[u] = __builtin_amdgcn_perm(in[2 * u + 1][jd >> 1], in[2 * u][jd >> 1], (jd & 1) ? 0x07060302u : 0x05040100u);
  return o;
}
__device__ __forceinline__ v4u frag_from_lanes(float v) {
  unsigned p = pk2(v, dppf<SHL1>(v));
  return v4u{p, dppu<SHL2>(p), dppu<SHL4>(p), dppu<SHL6>(p)};
}
__device__ __forceinline__ float silu_bf(float x) { return rbf(x / (1.f + __expf(-x))); }

#define STAMP(i) do { if ((i) == DIAGH) tmark = __builtin_amdgcn_s_memrealtime(); } while (0)

__global__ void __launch_bounds__(512, 2)
fused_kernel(const u16* __restrict__ x, const u16* __restrict__ kc, const u16* __restrict__ vc, const u16* __restrict__ wq,
             const u16* __restrict__ wo, const u16* __restrict__ w1, const u16* __restrict__ b1, const u16* __restrict__ w2,
             const u16* __restrict__ b2, const u16* __restrict__ cnd, const u16* __restrict__ wmod, u16* __restrict__ out,
             unsigned char* ws) {
  __shared__ __attribute__((aligned(16))) unsigned char lds[L_BYTES];
  const int tid = threadIdx.x, lane = tid & 63, w = __builtin_amdgcn_readfirstlane(tid >> 6);
  const int cb = (blockIdx.x & 7) * 32 + (blockIdx.x >> 3);
  const int grp = cb >> 6, sp = cb & 63, ri = cb >> 1;
  const bool isX = (cb & 1) == 0;
  const int r16 = lane & 15, g4 = lane >> 4;
  const unsigned long long t0 = __builtin_amdgcn_s_memrealtime();
  unsigned long long tmark = t0;
  const rsrc_t rW = mk(ws, WS_BYTES), rMod = mk(wmod, D * 6 * D * 2), rQ = mk(wq, D * 4096 * 2), rK = mk(kc, 4 * SK * DH * 2), rV = mk(vc, 4 * SK * DH * 2);
  const rsrc_t rC = mk(cnd, D * 2), rX = mk(x, D * 2);

  v4u pfc = {0, 0, 0, 0};
  {
    unsigned long long pc;
    asm volatile("s_getpc_b64 %0" : "=s"(pc));
    const rsrc_t rP = mk((const void*)(pc & ~255ull), CODE_BYTES);
    if (w == 7) pfc = ld128<0>(rP, lane * 16 + (blockIdx.x >> 3) * 1024, 0);
  }
  const int vo8 = (lane & 7) * 16;
  const int rg = lane >> 3;
  v4u f1w[2];
  {
    const int vo = (2 * rg) * 12288 + vo8, so = ((cb >> 5) * 128 + 16 * w) * 12288 + (cb & 31) * 128;
    f1w[0] = ld128<AUX_NT>(rMod, vo, so); f1w[1] = ld128<AUX_NT>(rMod, vo, so + 12288);
  }
  const unsigned c1 = ld32<0>(rC, rg * 4, ((cb >> 5) * 128 + 16 * w) * 2);
  const unsigned epoch = ld32<AUX_SC1>(rW, 0, WS_EPOCH);
  st128(v4u{SENT, SENT, SENT, SENT}, rW, tid * 16, WS_SENT + cb * 8192);
  const v4u xa = ld128<0>(rX, lane * 32, 0), xb = ld128<0>(rX, lane * 32, 16);
  const unsigned xh = ld32<0>(rX, r16 * 4, ((cb >> 6) * 256 + 32 * w) * 2);
  const v4u c3 = ld128<0>(rC, rg * 16, ((ri & 1) * 512 + 64 * w) * 2);
  SB();
  v4u f2w[4];
  {
    const int vo = (4 * rg) * 8192 + vo8, so = ((cb >> 6) * 256 + 32 * w) * 8192 + (cb & 63) * 128;
#pragma unroll
    for (int i = 0; i < 4; ++i) f2w[i] = ld128<AUX_NT>(rQ, vo, so + i * 8192);
  }
  SB();
  const unsigned tag = epoch + 1u;
  asm volatile("" ::"v"(pfc));
  {
    float acc[8] = {0, 0, 0, 0, 0, 0, 0, 0};
    fma8(acc, f1w[0], silu_bf(bflo(c1))); fma8(acc, f1w[1], silu_bf(bfhi(c1)));
    ((float*)(lds + L_F1))[w * 64 + 8 * (lane & 7) + rg] = reduce8(acc, lane);
  }
  float ln_m, ln_den;
  {
    float v[16];
#pragma unroll
    for (int j = 0; j < 4; ++j) { v[2 * j] = bflo(xa[j]); v[2 * j + 1] = bfhi(xa[j]); v[8 + 2 * j] = bflo(xb[j]); v[9 + 2 * j] = bfhi(xb[j]); }
    float s = 0.f;
#pragma unroll
    for (int j = 0; j < 16; ++j) s += v[j];
    const float mean = wave_sum(s) * (1.f / D);
    float q = 0.f;
#pragma unroll
    for (int j = 0; j < 16; ++j) { const float d = v[j] - mean; q = fmaf(d, d, q); }
    const float var = wave_sum(q) * (1.f / D);
    ln_m = rbf(mean); ln_den = rbf(sqrtf(rbf(rbf(var) + 1e-5f)));
  }
  v4u rw[8];
  const rsrc_t rR = isX ? mk(wo, 4096 * D * 2) : rMod;
  const int r_vo = isX ? g4 * 8 * 2048 + r16 * 16 : (8 * rg) * 12288 + vo8;
  const int r_so = isX ? ri * 65536 + w * 256 : ((ri & 1) * 512 + 64 * w) * 12288 + 4096 + (ri >> 1) * 128;
  const int r_st = isX ? 2048 : 12288;
  PACE(4);
  rw[0] = ld128<AUX_NT>(rR, r_vo, r_so);
  wg_barrier();
  if (w == 0) {
    const float* f = (const float*)(lds + L_F1);
    float s = 0.f;
#pragma unroll
    for (int i = 0; i < 8; ++i) s += f[i * 64 + lane];
    st64(v2u{f2u(s), tag}, rW, lane * 8, WS_G1 + ((cb & 31) * 8 + (cb >> 5)) * 512);
  }
  STAMP(1);
#pragma unroll
  for (int i = 1; i < 8; ++i) { PACE(WIN - 1); rw[i] = ld128<AUX_NT>(rR, r_vo, r_so + i * r_st); }
  SB();

  const int k_vo = (4 * (lane & 3) + ((lane >> 2) & 1)) * 256 + ((lane >> 3) & 1) * 64 + g4 * 16;
  const int kv_so = cb * 262144 + w * 4096;
  v4u kv[32];
#define KLOADN(n, N) do { PACE(N); kv[n] = ld128<AUX_NT>(rK, k_vo + (((n) >> 1) & 1) * 512 + ((n) & 1) * 128, kv_so + ((n) >> 2) * 32768); SB(); } while (0)
#define VLOADN(n, N) do { PACE(N); vv[n] = ld128<AUX_NT>(rV, lane * 16 + ((n) & 3) * 1024, kv_so + ((n) >> 2) * 32768); SB(); } while (0)
#define KLOAD(n) KLOADN(n, WIN - 1)
#define VLOAD(n) VLOADN(n, WIN - 1)
  KLOAD(0); KLOAD(1); KLOAD(2); KLOAD(3);
  if (isX) {
#pragma unroll
    for (int jd = 0; jd < 8; ++jd) *(v4u*)(lds + L_STASH + (w * 8 + jd) * 1024 + lane * 16) = tr8(rw, jd);
  } else {
    float acc[8] = {0, 0, 0, 0, 0, 0, 0, 0};
#pragma unroll
    for (int i = 0; i < 8; ++i) fma8(acc, rw[i], silu_bf((i & 1) ? bfhi(c3[i >> 1]) : bflo(c3[i >> 1])));
    ((float*)(lds + L_F3))[w * 64 + 8 * (lane & 7) + rg] = reduce8(acc, lane);
  }
  SB();
  {
    const int e0 = (cb >> 6) * 256 + 32 * w;
    const int g_vo = ((g4 & 1) * 16 * 8 + (g4 >> 1) * 4) * 512 + r16 * 16;
    const int g_so = WS_G1 + (e0 >> 6) * 4096 + (e0 & 63) * 8;
    v4u g[4];
#pragma unroll
    for (int i = 0; i < 4; ++i) g[i] = ld128<AUX_SC1>(rW, g_vo + i * 512, g_so);
    SB();
    KLOADN(4, WIN + 3); KLOADN(5, WIN + 3); KLOADN(6, WIN + 3); KLOADN(7, WIN + 3);
    if (wave_any(TAGBAD(g))) {
      do {
        CFENCE();
        g[0] = ld128<AUX_SC1>(rW, g_vo, g_so); g[1] = ld128<AUX_SC1>(rW, g_vo + 512, g_so);
        g[2] = ld128<AUX_SC1>(rW, g_vo + 1024, g_so); g[3] = ld128<AUX_SC1>(rW, g_vo + 1536, g_so);
      } while (wave_any(TAGBAD(g)) && !expired(t0));
    }
    STAMP(2);
    float s0 = (u2f(g[0].x) + u2f(g[1].x)) + (u2f(g[2].x) + u2f(g[3].x)), s1 = (u2f(g[0].z) + u2f(g[1].z)) + (u2f(g[2].z) + u2f(g[3].z));
    s0 = rbf(sum_sw32(s0, s0)); s1 = rbf(sum_sw32(s1, s1));
    auto e0s = __builtin_amdgcn_permlane16_swap(f2u(s0), f2u(s0), false, false);
    auto e1s = __builtin_amdgcn_permlane16_swap(f2u(s1), f2u(s1), false, false);
    const float h0 = rbf(rbf(rbf(rbf(bflo(xh) - ln_m) / ln_den) * rbf(1.f + u2f(e0s[1]))) + u2f(e0s[0]));
    const float h1 = rbf(rbf(rbf(rbf(bfhi(xh) - ln_m) / ln_den) * rbf(1.f + u2f(e1s[1]))) + u2f(e1s[0]));
    if (lane < 16) *(f32x2*)(lds + L_H + w * 128 + r16 * 8) = f32x2{h0, h1};
    asm volatile("s_waitcnt lgkmcnt(0)" ::: "memory");
    const f32x4 hv = *(const f32x4*)(lds + L_H + w * 128 + rg * 16);
    float acc[8] = {0, 0, 0, 0, 0, 0, 0, 0};
#pragma unroll
    for (int i = 0; i < 4; ++i) fma8(acc, f2w[i], hv[i]);
    ((float*)(lds + L_F2))[w * 64 + 8 * (lane & 7) + rg] = reduce8(acc, lane);
  }
  KLOAD(8);
  wg_barrier();
  if (w == 0 || (w == 1 && !isX)) {
    const float* f = (const float*)(lds + (w == 0 ? L_F2 : L_F3));
    float s = 0.f;
#pragma unroll
    for (int i = 0; i < 8; ++i) s += f[i * 64 + lane];
    st64(v2u{f2u(s), tag}, rW, lane * 8, w == 0 ? WS_G2 + ((cb & 63) * 4 + (cb >> 6)) * 512 : WS_G3 + ri * 512);
  }
  STAMP(3);
  KLOAD(9); KLOAD(10); KLOAD(11); KLOAD(12); KLOAD(13); KLOAD(14); KLOAD(15);
  KLOAD(16); KLOAD(17); KLOAD(18); KLOAD(19); KLOAD(20); KLOAD(21); KLOAD(22); KLOAD(23);
  {
    const int g_vo = (tid >> 5) * 2048 + (tid & 31) * 16, g_so = WS_G2 + grp * 32768;
    v4u g[4];
#pragma unroll
    for (int i = 0; i < 4; ++i) g[i] = ld128<AUX_SC1>(rW, g_vo + i * 512, g_so);
    SB();
    KLOADN(24, WIN + 3); KLOADN(25, WIN + 3); KLOADN(26, WIN + 3); KLOADN(27, WIN + 3);
    if (wave_any(TAGBAD(g))) {
      do {
        CFENCE();
        g[0] = ld128<AUX_SC1>(rW, g_vo, g_so); g[1] = ld128<AUX_SC1>(rW, g_vo + 512, g_so);
        g[2] = ld128<AUX_SC1>(rW, g_vo + 1024, g_so); g[3] = ld128<AUX_SC1>(rW, g_vo + 1536, g_so);
      } while (wave_any(TAGBAD(g)) && !expired(t0));
    }
    const float s0 = (u2f(g[0].x) + u2f(g[1].x)) + (u2f(g[2].x) + u2f(g[3].x)), s1 = (u2f(g[0].z) + u2f(g[1].z)) + (u2f(g[2].z) + u2f(g[3].z));
    *(unsigned*)(lds + L_Q + tid * 4) = pk2(s0, s1);
  }
  KLOAD(28);
  wg_barrier();
  STAMP(4);
  if (cb == 0 && tid == 0) __builtin_amdgcn_raw_buffer_store_b32(tag, rW, 0, WS_EPOCH, AUX_SC1);
  KLOAD(29); KLOAD(30); KLOAD(31);
  const v4u q0 = *(const v4u*)(lds + L_Q + (r16 & 7) * 256 + (r16 >> 3) * 64 + g4 * 16);
  const v4u q1 = *(const v4u*)(lds + L_Q + (r16 & 7) * 256 + (r16 >> 3) * 64 + g4 * 16 + 128);

  v4u vv[32];
  float sc[32];
  float mx = -INFINITY;
#define QK(j) do { \
    f32x4 d0 = mfma(kv[4 * (j)], q0, f32x4{0, 0, 0, 0}); d0 = mfma(kv[4 * (j) + 1], q1, d0); \
    f32x4 d1 = mfma(kv[4 * (j) + 2], q0, f32x4{0, 0, 0, 0}); d1 = mfma(kv[4 * (j) + 3], q1, d1); \
    _Pragma("unroll") for (int i = 0; i < 4; ++i) { \
      auto r_ = __builtin_amdgcn_permlane32_swap(f2u(d0[i]), f2u(d1[i]), false, false); \
      const float s_ = rbf(rbf(u2f(r_[0]) + dppf<ROR8>(u2f(r_[1]))) * 0.08838834764831845f); \
      sc[4 * (j) + i] = s_; mx = fmaxf(mx, s_); } \
    SB(); } while (0)
  QK(0); VLOAD(0); QK(1); QK(2); VLOAD(1); QK(3); QK(4); VLOAD(2); QK(5); QK(6); VLOAD(3); QK(7);
  {
    mx = max_sw32(max_sw16(mx));
    float l = 0.f;
#pragma unroll
    for (int i = 0; i < 32; ++i) l += __expf(sc[i] - mx);
    l = sum_sw16(l, l); l = sum_sw32(l, l);
    if (lane < 8) *(f32x2*)(lds + L_ST + w * 64 + lane * 8) = f32x2{mx, l};
  }
  VLOAD(4);
  wg_barrier();
  if (w == 0) {
    const f32x2 ml = *(const f32x2*)(lds + L_ST + lane * 8);
    float M = fmaxf(ml[0], dppf<ROR8>(ml[0])); M = max_sw32(max_sw16(M));
    float L = ml[1] * __expf(ml[0] - M);
    L += dppf<ROR8>(L); L = sum_sw16(L, L); L = sum_sw32(L, L);
    if (lane < 8) st64(v2u{nosent(M), nosent(L)}, rW, lane * 8, WS_ST + cb * 64);
  }
  STAMP(5);
  VLOAD(5); VLOAD(6); VLOAD(7); VLOAD(8); VLOAD(9); VLOAD(10); VLOAD(11);
  const bool isRed = isX && (ri & 3) == 0;
  const int rj = ri >> 2;
  float m_a0, m_a1, m_b0, m_b1;
  {
    const int ea = isX ? 32 * rj + (lane & 30) : 2048 + 2 * tid, eb = isX ? 3072 + 32 * rj + (lane & 30) : 1024 + 2 * tid;
    const int va = (ea >> 6) * 1024 + (ea & 63) * 8, vb = (eb >> 6) * 1024 + (eb & 63) * 8;
    v4u g[4];
    g[0] = ld128<AUX_SC1>(rW, va, WS_G3); g[1] = ld128<AUX_SC1>(rW, va + 512, WS_G3);
    g[2] = ld128<AUX_SC1>(rW, vb, WS_G3); g[3] = ld128<AUX_SC1>(rW, vb + 512, WS_G3);
    SB();
    VLOADN(12, WIN + 3); VLOADN(13, WIN + 3); VLOADN(14, WIN + 3); VLOADN(15, WIN + 3);
    if (wave_any(TAGBAD(g))) {
      do {
        CFENCE();
        g[0] = ld128<AUX_SC1>(rW, va, WS_G3); g[1] = ld128<AUX_SC1>(rW, va + 512, WS_G3);
        g[2] = ld128<AUX_SC1>(rW, vb, WS_G3); g[3] = ld128<AUX_SC1>(rW, vb + 512, WS_G3);
      } while (wave_any(TAGBAD(g)) && !expired(t0));
    }
    m_a0 = rbf(u2f(g[0].x) + u2f(g[1].x)); m_a1 = rbf(u2f(g[0].z) + u2f(g[1].z));
    m_b0 = rbf(u2f(g[2].x) + u2f(g[3].x)); m_b1 = rbf(u2f(g[2].z) + u2f(g[3].z));
    if (isX) { if (lane & 1) { m_a0 = m_a1; m_b0 = m_b1; } }
    else { m_a0 = rbf(1.f + m_a0); m_a1 = rbf(1.f + m_a1); }
  }
  VLOAD(16); VLOAD(17); VLOAD(18); VLOAD(19);
  unsigned pf[16];
  {
    v2u g[8];
#pragma unroll
    for (int i = 0; i < 8; ++i) g[i] = ld64<AUX_SC1>(rW, lane * 8 + i * 512, WS_ST + grp * 4096);
    SB();
    VLOADN(20, WIN + 7); VLOADN(21, WIN + 7); VLOADN(22, WIN + 7); VLOADN(23, WIN + 7);
#define M2(i) max(g[i].x, g[i].y)
#define STBAD() (max(max(max(M2(0), M2(1)), max(M2(2), M2(3))), max(max(M2(4), M2(5)), max(M2(6), M2(7)))) == SENT)
#define STLD(i) g[i] = ld64<AUX_SC1>(rW, lane * 8 + (i) * 512, WS_ST + grp * 4096)
    if (wave_any(STBAD())) {
      do {
        CFENCE();
        STLD(0); STLD(1); STLD(2); STLD(3); STLD(4); STLD(5); STLD(6); STLD(7);
      } while (wave_any(STBAD()) && !expired(t0));
    }
    STAMP(6);
    float M = u2f(g[0].x);
#pragma unroll
    for (int i = 1; i < 8; ++i) M = fmaxf(M, u2f(g[i].x));
    M = fmaxf(M, dppf<ROR8>(M)); M = max_sw32(max_sw16(M));
    float L = 0.f;
#pragma unroll
    for (int i = 0; i < 8; ++i) L = fmaf(u2f(g[i].y), __expf(u2f(g[i].x) - M), L);
    L += dppf<ROR8>(L); L = sum_sw16(L, L); L = sum_sw32(L, L);
    const float rL = 1.f / L;
#pragma unroll
    for (int i = 0; i < 16; ++i) pf[i] = pk2(__expf(sc[2 * i] - M) * rL, __expf(sc[2 * i + 1] - M) * rL);
  }
  f32x4 acc[8];
#pragma unroll
  for (int jd = 0; jd < 8; ++jd) acc[jd] = f32x4{0, 0, 0, 0};
#define PV(t) do { \
    const v4u pa_ = {pf[4 * (t)], pf[4 * (t) + 1], pf[4 * (t) + 2], pf[4 * (t) + 3]}; \
    _Pragma("unroll") for (int jd = 0; jd < 8; ++jd) acc[jd] = mfma(pa_, tr8(&vv[8 * (t)], jd), acc[jd]); \
    SB(); } while (0)
  VLOAD(24); PV(0); VLOAD(25); VLOAD(26); PV(1); VLOAD(27); VLOAD(28); PV(2); VLOAD(29); VLOAD(30); VLOAD(31);
  PV(3);
  if (lane < 32) {
#pragma unroll
    for (int i = 0; i < 4; ++i) {
      float* p = (float*)(lds + L_RED + w * 4096 + (4 * g4 + i) * 512 + r16 * 16);
      *(f32x4*)p = f32x4{acc[0][i], acc[1][i], acc[2][i], acc[3][i]};
      *(f32x4*)(p + 64) = f32x4{acc[4][i], acc[5][i], acc[6][i], acc[7][i]};
    }
  }
  wg_barrier();
  if (w < 4) {
    const int head = tid >> 5, idx = tid & 31;
    const unsigned char* p = lds + L_RED + head * 512 + (idx & 1) * 256 + (idx >> 1) * 16;
    f32x4 s = *(const f32x4*)p;
#pragma unroll
    for (int i = 1; i < 8; ++i) s += *(const f32x4*)(p + i * 4096);
    st128(v4u{nosent(s[0]), nosent(s[1]), nosent(s[2]), nosent(s[3])}, rW, (head * 4 + (idx >> 3)) * STR_A + (idx & 7) * 16, WS_PA + grp * 32 * STR_A + sp * 128);
  }
  STAMP(7);
  const int ecomp = 4 * (lane & 7) + (g4 & 1) * 2 + (g4 >> 1);
  const int efrag = 8 * g4 + (lane & 7);
  const int out_vo = (r16 & 3) * 32 + (g4 >> 1) * 16;

  if (isX) {
    v4u wf[8];
#pragma unroll
    for (int jd = 0; jd < 8; ++jd) wf[jd] = *(const v4u*)(lds + L_STASH + (w * 8 + jd) * 1024 + lane * 16);
    unsigned xr = 0, b2r = 0;
    if (isRed) {
      xr = __builtin_amdgcn_raw_buffer_load_b16(rX, (lane & 31) * 2, rj * 64, 0);
      b2r = __builtin_amdgcn_raw_buffer_load_b16(mk(b2, D * 2), (lane & 31) * 2, rj * 64, 0);
    }
    v4u g;
    for (;;) {
      g = ld128<AUX_SC1>(rW, tid * 16, WS_PA + ri * STR_A);
      if (!wave_any(has_sent(g)) || expired(t0)) break;
      CFENCE();
    }
    const float part = reduce4(__builtin_bit_cast(f32x4, g));
    if (!(lane & 8)) ((float*)(lds + L_T1))[ecomp * 12 + w] = part;
    wg_barrier();
    STAMP(8);
    {
      const f32x4 a = *(const f32x4*)(lds + L_T1 + efrag * 48), b = *(const f32x4*)(lds + L_T1 + efrag * 48 + 16);
      const float o = ((a[0] + a[1]) + (a[2] + a[3])) + ((b[0] + b[1]) + (b[2] + b[3]));
      const v4u af = frag_from_lanes(o);
      f32x4 d[8];
#pragma unroll
      for (int jd = 0; jd < 8; ++jd) d[jd] = mfma(af, wf[jd], f32x4{0, 0, 0, 0});
      const bool hi = lane >= 32;
      const v4u o4 = {nosent(hi ? d[4][0] : d[0][0]), nosent(hi ? d[5][0] : d[1][0]), nosent(hi ? d[6][0] : d[2][0]), nosent(hi ? d[7][0] : d[3][0])};
      if (!(g4 & 1)) st128(o4, rW, (r16 >> 2) * STR_B + out_vo, WS_PB + 4 * w * STR_B + ri * 128);
    }
    STAMP(9);
    if (isRed) {
      v4u ga, gb;
      for (;;) {
        ga = ld128<AUX_SC1>(rW, tid * 16, WS_PB + rj * STR_B); gb = ld128<AUX_SC1>(rW, tid * 16, WS_PB + rj * STR_B + 8192);
        if (!wave_any(has_sent(ga) | has_sent(gb)) || expired(t0)) break;
        CFENCE();
      }
      const float p2 = reduce4(__builtin_bit_cast(f32x4, ga) + __builtin_bit_cast(f32x4, gb));
      if (!(lane & 8)) ((float*)(lds + L_T2))[ecomp * 12 + w] = p2;
      wg_barrier();
      STAMP(10);
      float x2 = 0.f;
      if (w == 0) {
        const f32x4 a = *(const f32x4*)(lds + L_T2 + (lane & 31) * 48), b = *(const f32x4*)(lds + L_T2 + (lane & 31) * 48 + 16);
        const float at = rbf(((a[0] + a[1]) + (a[2] + a[3])) + ((b[0] + b[1]) + (b[2] + b[3])));
        x2 = rbf(bflo(xr) + rbf(m_a0 * at));
        v4u f = frag_from_lanes(x2);
        f = v4u{min(f.x, 0xFFFFFFFEu), min(f.y, 0xFFFFFFFEu), min(f.z, 0xFFFFFFFEu), min(f.w, 0xFFFFFFFEu)};
        if (lane < 32 && !(lane & 7)) st128(f, rW, lane * 2, WS_X2 + rj * 64);
      }
      STAMP(11);
      gb = v4u{0, 0, 0, 0};
      for (;;) {
        ga = ld128<AUX_SC1>(rW, tid * 16, WS_PD + rj * STR_D);
        if (w < 6) gb = ld128<AUX_SC1>(rW, tid * 16, WS_PD + rj * STR_D + 8192);
        if (!wave_any(has_sent(ga) | has_sent(gb)) || expired(t0)) break;
        CFENCE();
      }
      const float p3 = reduce4(__builtin_bit_cast(f32x4, ga) + __builtin_bit_cast(f32x4, gb));
      if (!(lane & 8)) ((float*)(lds + L_T3))[ecomp * 12 + w] = p3;
      wg_barrier();
      STAMP(14);
      if (w == 0) {
        const f32x4 a = *(const f32x4*)(lds + L_T3 + (lane & 31) * 48), b = *(const f32x4*)(lds + L_T3 + (lane & 31) * 48 + 16);
        const float mo = rbf(((a[0] + a[1]) + (a[2] + a[3])) + ((b[0] + b[1]) + (b[2] + b[3])));
        const float u = rbf(mo + bflo(b2r));
        const float res = x2 + rbf(m_b0 * u);
        const v4u f = frag_from_lanes(res);
        if (lane < 32 && !(lane & 7)) *(v4u*)(out + rj * 32 + lane) = f;
      }
      STAMP(15);
    }
  } else if (ri < 112) {
    unsigned w1r[32];
    v4u w2r[8];
    {
      const rsrc_t r1 = mk(w1, D * FF * 2), r2 = mk(w2, FF * D * 2);
      const int vo1 = g4 * 8 * (FF * 2) + r16 * 4, so1 = 128 * w * (FF * 2) + ri * 64;
#pragma unroll
      for (int i = 0; i < 32; ++i) w1r[i] = ld32<0>(r1, vo1, so1 + ((i >> 3) * 32 + (i & 7)) * (FF * 2));
#pragma unroll
      for (int j = 0; j < 8; ++j) w2r[j] = ld128<AUX_NT>(r2, g4 * 8 * 2048 + r16 * 16, ri * 65536 + w * 256 + j * 2048);
    }
    const unsigned b1r = __builtin_amdgcn_raw_buffer_load_b16(mk(b1, FF * 2), efrag * 2, ri * 64, 0);
    SB();
    v4u f1[4][2], f2[8];
#pragma unroll
    for (int ks = 0; ks < 4; ++ks)
#pragma unroll
      for (int u = 0; u < 4; ++u) {
        f1[ks][0][u] = __builtin_amdgcn_perm(w1r[8 * ks + 2 * u + 1], w1r[8 * ks + 2 * u], 0x05040100u);
        f1[ks][1][u] = __builtin_amdgcn_perm(w1r[8 * ks + 2 * u + 1], w1r[8 * ks + 2 * u], 0x07060302u);
      }
#pragma unroll
    for (int jd = 0; jd < 8; ++jd) f2[jd] = tr8(w2r, jd);
    STAMP(16);
    unsigned xp;
    for (;;) {
      xp = ld32<AUX_SC1>(rW, tid * 4, WS_X2);
      if (!wave_any(xp == SENT) || expired(t0)) break;
      CFENCE();
    }
    STAMP(12);
    float v0 = bflo(xp), v1 = bfhi(xp);
#pragma unroll
    for (int pass = 0; pass < 2; ++pass) {
      const float piv = u2f(__builtin_amdgcn_readfirstlane(f2u(v0)));
      const float d0 = v0 - piv, d1 = v1 - piv;
      const float s1 = wave_sum(d0 + d1), s2 = wave_sum(fmaf(d0, d0, d1 * d1));
      unsigned char* lp = lds + (pass ? L_LN2 : L_LN1);
      if (lane == 0) *(f32x2*)(lp + w * 8) = f32x2{piv + s1 * (1.f / 128), s2 - s1 * s1 * (1.f / 128)};
      wg_barrier();
      f32x4 st[4];
#pragma unroll
      for (int i = 0; i < 4; ++i) st[i] = *(const f32x4*)(lp + i * 16);
      const float mean = (((st[0][0] + st[0][2]) + (st[1][0] + st[1][2])) + ((st[2][0] + st[2][2]) + (st[3][0] + st[3][2]))) * 0.125f;
      float m2 = ((st[0][1] + st[0][3]) + (st[1][1] + st[1][3])) + ((st[2][1] + st[2][3]) + (st[3][1] + st[3][3])), dv = 0.f;
#pragma unroll
      for (int i = 0; i < 4; ++i) { const float a = st[i][0] - mean, b = st[i][2] - mean; dv = fmaf(a, a, fmaf(b, b, dv)); }
      const float var = fmaxf(fmaf(dv, 128.f, m2), 0.f) * (1.f / D);
      const float mb = rbf(mean), den = rbf(sqrtf(rbf(rbf(var) + 1e-5f)));
      v0 = rbf(rbf(v0 - mb) / den); v1 = rbf(rbf(v1 - mb) / den);
      if (pass == 0) { v0 = rbf(rbf(v0 * m_a0) + m_b0); v1 = rbf(rbf(v1 * m_a1) + m_b1); }
    }
    *(unsigned*)(lds + L_H + tid * 4) = pk2(v0, v1);
    asm volatile("s_waitcnt lgkmcnt(0)" ::: "memory");
    f32x4 h0 = {0, 0, 0, 0}, h1 = {0, 0, 0, 0};
#pragma unroll
    for (int ks = 0; ks < 4; ++ks) {
      const v4u ya = *(const v4u*)(lds + L_H + w * 256 + ks * 64 + g4 * 16);
      h0 = mfma(ya, f1[ks][0], h0); h1 = mfma(ya, f1[ks][1], h1);
    }
    if (lane < 16) { ((float*)(lds + L_T1))[(2 * lane) * 12 + w] = h0[0]; ((float*)(lds + L_T1))[(2 * lane + 1) * 12 + w] = h1[0]; }
    wg_barrier();
    {
      const f32x4 a = *(const f32x4*)(lds + L_T1 + efrag * 48), b = *(const f32x4*)(lds + L_T1 + efrag * 48 + 16);
      const float t = rbf(rbf(((a[0] + a[1]) + (a[2] + a[3])) + ((b[0] + b[1]) + (b[2] + b[3]))) + bflo(b1r));
      const float inner = 0.7978845608028654f * (t + 0.044715f * t * t * t);
      const float th = 1.f - 2.f / (1.f + __expf(2.f * inner));
      const v4u af = frag_from_lanes(0.5f * t * (1.f + th));
      f32x4 d[8];
#pragma unroll
      for (int jd = 0; jd < 8; ++jd) d[jd] = mfma(af, f2[jd], f32x4{0, 0, 0, 0});
      const bool hi = lane >= 32;
      const v4u o4 = {nosent(hi ? d[4][0] : d[0][0]), nosent(hi ? d[5][0] : d[1][0]), nosent(hi ? d[6][0] : d[2][0]), nosent(hi ? d[7][0] : d[3][0])};
      if (!(g4 & 1)) st128(o4, rW, (r16 >> 2) * STR_D + out_vo, WS_PD + 4 * w * STR_D + ri * 128);
    }
    STAMP(13);
  }
  if (cb == 0 && w == 0) { const unsigned long long until = t0 + 6000ull + 4ull * (tmark - t0); while (__builtin_amdgcn_s_memrealtime() < until) __builtin_amdgcn_s_sleep(1); }
}

}

extern "C" void kernel_launch(void* const* d_in, const int* in_sizes, int n_in, void* d_out, int out_size, void* d_ws, size_t ws_size,
                              hipStream_t stream) {
  (void)in_sizes; (void)n_in; (void)out_size; (void)ws_size;
  fused_kernel<<<dim3(256), dim3(512), 0, stream>>>(
      (const u16*)d_in[0], (const u16*)d_in[1], (const u16*)d_in[2], (const u16*)d_in[3], (const u16*)d_in[4], (const u16*)d_in[6],
      (const u16*)d_in[7], (const u16*)d_in[8], (const u16*)d_in[9], (const u16*)d_in[10], (const u16*)d_in[11], (u16*)d_out,
      (unsigned char*)d_ws);
}
```

```cpp
#include <hip/hip_runtime.h>
#include <hip/hip_bf16.h>
#include <stdint.h>

typedef unsigned short u16;
typedef unsigned v4u __attribute__((ext_vector_type(4)));
typedef unsigned v2u __attribute__((ext_vector_type(2)));
typedef int v4i __attribute__((ext_vector_type(4)));
typedef short v4s __attribute__((ext_vector_type(4)));
typedef float f32x4 __attribute__((ext_vector_type(4)));
typedef float f32x2 __attribute__((ext_vector_type(2)));
typedef short bf16x8 __attribute__((ext_vector_type(8)));
typedef __bf16 bf16x2_t __attribute__((ext_vector_type(2)));
typedef __amdgpu_buffer_rsrc_t rsrc_t;
#define LAS __attribute__((address_space(3)))

namespace {

constexpr int D = 1024, SK = 65536, DH = 128, FF = 3584;
constexpr int AUX_NT = 2, AUX_SC1 = 16;
constexpr unsigned SENT = 0xFFFFFFFFu;
constexpr int CODE_BYTES = 21 * 1024;
constexpr unsigned SPIN_CAP = 1u << 17;
constexpr float LOG2E = 1.4426950408889634f;

#define TAU2 27
#define TH1 0
#define TH2 40
#define WLO 5
#define WMID 7
#define WHI 13
#define TOFF 400

constexpr int WS_EPOCH = 0;
constexpr int WS_G1 = 4096;
constexpr int WS_G2 = WS_G1 + 131072;
constexpr int WS_G3 = WS_G2 + 65536;
constexpr int WS_SENT = 512 * 1024;
constexpr int STR_A = 8192 + 128, STR_B = 16384 + 14336 + 128;
constexpr int WS_PA = WS_SENT;
constexpr int WS_PB = WS_PA + 128 * STR_A;
constexpr int WS_PD = WS_PB + 16384;
constexpr int WS_ST = WS_PB + 32 * STR_B;
constexpr int WS_U = WS_ST + 32768;
#define XS_STR 32
constexpr int WS_XS = WS_U + 4096;
constexpr int WS_SENT_END = WS_XS + 32 * XS_STR;
static_assert(WS_SENT_END <= WS_SENT + 2 * 1024 * 1024, "sentinel region");
constexpr int WS_DIAG = WS_SENT + 2 * 1024 * 1024;
constexpr int WS_BYTES = WS_DIAG + 256 * 64 * 8;

constexpr int U_K = 0, U_V = 64, U_W = 128, U_WEND = 144;
#define TAU2K 37
#define GATE_Q 34
#define GATE_G3 (U_V + 24)
#define GATE_ST (U_V + 40)
#define F2POS 2
#define PRL 0
#define PRS 3
#define PRW 2
#define W1AUX 0
#define SLPC 1
#define KPARK 4
#define VPARK 16
#define WPRE 4

constexpr int L_S = 131072;
constexpr int L_Q = L_S + 16384;
constexpr int L_F1 = L_Q + 2048;
constexpr int L_F2 = L_F1 + 2048;
constexpr int L_F3 = L_F2 + 2048;
constexpr int L_H = L_F3 + 2048;
constexpr int L_CS = L_H + 2048;
constexpr int L_ST = L_CS + 4096;
constexpr int L_CNT = L_ST + 512;
constexpr int C_F1 = L_CNT, C_F2 = L_CNT + 4, C_F3 = L_CNT + 8, C_Q = L_CNT + 12, C_ST = L_CNT + 16, C_CS = L_CNT + 20, C_P = L_CNT + 24,
              C_ML = L_CNT + 28, C_SILU = L_CNT + 32, C_PUB = L_CNT + 36, C_PRE = L_CNT + 40;
constexpr int L_FULL = L_CNT + 48, L_PROG = L_CNT + 64;
constexpr int L_BYTES = L_CNT + 128;

__device__ __forceinline__ float u2f(unsigned u) { return __builtin_bit_cast(float, u); }
__device__ __forceinline__ unsigned f2u(float f) { return __builtin_bit_cast(unsigned, f); }
__device__ __forceinline__ float bflo(unsigned p) { return u2f(p << 16); }
__device__ __forceinline__ float bfhi(unsigned p) { return u2f(p & 0xffff0000u); }
__device__ __forceinline__ unsigned pk2(float lo, float hi) {
  f32x2 v = {lo, hi};
  return __builtin_bit_cast(unsigned, __builtin_convertvector(v, bf16x2_t));
}
__device__ __forceinline__ float rbf(float f) { return bflo(pk2(f, f)); }
template <int CTRL> __device__ __forceinline__ unsigned dppu(unsigned x) { return (unsigned)__builtin_amdgcn_mov_dpp((int)x, CTRL, 0xf, 0xf, true); }
template <int CTRL> __device__ __forceinline__ float dppf(float x) { return u2f(dppu<CTRL>(f2u(x))); }
constexpr int XOR1 = 0xB1, XOR2 = 0x4E, XOR7 = 0x141, ROR8 = 0x128, SHL1 = 0x101, SHL2 = 0x102, SHL4 = 0x104, SHL6 = 0x106;
__device__ __forceinline__ float sum_sw32(float a, float b) { auto r = __builtin_amdgcn_permlane32_swap(f2u(a), f2u(b), false, false); return u2f(r[0]) + u2f(r[1]); }
__device__ __forceinline__ float sum_sw16(float a, float b) { auto r = __builtin_amdgcn_permlane16_swap(f2u(a), f2u(b), false, false); return u2f(r[0]) + u2f(r[1]); }
__device__ __forceinline__ float max_sw32(float a) { auto r = __builtin_amdgcn_permlane32_swap(f2u(a), f2u(a), false, false); return fmaxf(u2f(r[0]), u2f(r[1])); }
__device__ __forceinline__ float max_sw16(float a) { auto r = __builtin_amdgcn_permlane16_swap(f2u(a), f2u(a), false, false); return fmaxf(u2f(r[0]), u2f(r[1])); }
__device__ __forceinline__ float half_sum(float v) {
  v += dppf<XOR1>(v); v += dppf<XOR2>(v); v += dppf<XOR7>(v); v += dppf<ROR8>(v);
  return sum_sw16(v, v);
}
__device__ __forceinline__ float wave_sum(float v) { v = half_sum(v); return sum_sw32(v, v); }
__device__ __forceinline__ float max_hi3(float v) { v = fmaxf(v, dppf<ROR8>(v)); return max_sw32(max_sw16(v)); }
__device__ __forceinline__ float wave_max(float v) { v = fmaxf(v, dppf<XOR1>(v)); v = fmaxf(v, dppf<XOR2>(v)); v = fmaxf(v, dppf<XOR7>(v)); return max_hi3(v); }
__device__ __forceinline__ float sum_hi3(float v) { v += dppf<ROR8>(v); v = sum_sw16(v, v); return sum_sw32(v, v); }
__device__ __forceinline__ f32x4 mfma(v4u a, v4u b, f32x4 c) {
  return __builtin_amdgcn_mfma_f32_16x16x32_bf16(__builtin_bit_cast(bf16x8, a), __builtin_bit_cast(bf16x8, b), c, 0, 0, 0);
}
__device__ __forceinline__ rsrc_t mk(const void* p, int bytes) { return __builtin_amdgcn_make_buffer_rsrc((void*)p, 0, bytes, 0x00020000); }
template <int AUX> __device__ __forceinline__ v4u ld128(rsrc_t r, int vo, int so) { return __builtin_amdgcn_raw_buffer_load_b128(r, vo, so, AUX); }
template <int AUX> __device__ __forceinline__ v2u ld64(rsrc_t r, int vo, int so) { return __builtin_amdgcn_raw_buffer_load_b64(r, vo, so, AUX); }
template <int AUX> __device__ __forceinline__ unsigned ld32(rsrc_t r, int vo, int so) { return __builtin_amdgcn_raw_buffer_load_b32(r, vo, so, AUX); }
__device__ __forceinline__ unsigned ld16(rsrc_t r, int vo, int so) { return __builtin_amdgcn_raw_buffer_load_b16(r, vo, so, 0); }
__device__ __forceinline__ void st128(v4u v, rsrc_t r, int vo, int so) { __builtin_amdgcn_raw_buffer_store_b128(v, r, vo, so, AUX_SC1); }
__device__ __forceinline__ void st64(v2u v, rsrc_t r, int vo, int so) { __builtin_amdgcn_raw_buffer_store_b64(v, r, vo, so, AUX_SC1); }
#define SB() __builtin_amdgcn_sched_barrier(0)
#define CFENCE() asm volatile("" ::: "memory")
#define LGKM0() asm volatile("s_waitcnt lgkmcnt(0)" ::: "memory")
#define VM(N) asm volatile("s_waitcnt vmcnt(%0)" ::"i"(N) : "memory")
#define PIN(a) asm volatile("" : "+v"(a))
__device__ __forceinline__ void wg_barrier() {
  LGKM0(); SB();
  __builtin_amdgcn_s_barrier();
  CFENCE(); SB();
}
__device__ __forceinline__ unsigned nosentu(unsigned v) { return min(v, 0xFFFFFFFEu); }
__device__ __forceinline__ unsigned nosent(float v) { return nosentu(f2u(v)); }
__device__ __forceinline__ unsigned max4(v4u v) { return max(max(v.x, v.y), max(v.z, v.w)); }
__device__ __forceinline__ bool wave_any(bool p) { return __builtin_amdgcn_ballot_w64(p) != 0ull; }
__device__ __forceinline__ float rcp(float x) { return __builtin_amdgcn_rcpf(x); }
__device__ __forceinline__ float ex2(float x) { return __builtin_amdgcn_exp2f(x); }
__device__ __forceinline__ float silu_bf(float x) { return rbf(x / (1.f + expf(-x))); }
__device__ __forceinline__ float ln_rstd(float var) { return rcp(rbf(__builtin_amdgcn_sqrtf(rbf(rbf(var) + 1e-5f)))); }

__device__ __forceinline__ void fma8(float (&acc)[8], v4u w, float x) {
#pragma unroll
  for (int j = 0; j < 4; ++j) { acc[2 * j] = fmaf(x, bflo(w[j]), acc[2 * j]); acc[2 * j + 1] = fmaf(x, bfhi(w[j]), acc[2 * j + 1]); }
}
__device__ __forceinline__ float reduce8(const float (&a)[8], int lane) {
  float b0 = sum_sw32(a[0], a[4]), b1 = sum_sw32(a[1], a[5]), b2 = sum_sw32(a[2], a[6]), b3 = sum_sw32(a[3], a[7]);
  float c0 = sum_sw16(b0, b2), c1 = sum_sw16(b1, b3);
  const bool b = lane & 8;
  float keep = b ? c1 : c0, send = b ? c0 : c1;
  return keep + dppf<ROR8>(send);
}
__device__ __forceinline__ float reduce4(f32x4 v) {
  float a = sum_sw32(v[0], v[1]), b = sum_sw32(v[2], v[3]);
  float c = sum_sw16(a, b);
  return c + dppf<ROR8>(c);
}
__device__ __forceinline__ v4u tr8(const v4u* in, int jd) {
  v4u o;
#pragma unroll
  for (int u = 0; u < 4; ++u) o[u] = __builtin_amdgcn_perm(in[2 * u + 1][jd >> 1], in[2 * u][jd >> 1], (jd & 1) ? 0x07060302u : 0x05040100u);
  return o;
}
__device__ __forceinline__ v4u frag_from_lanes(float v) {
  unsigned p = pk2(v, dppf<SHL1>(v));
  return v4u{p, dppu<SHL2>(p), dppu<SHL4>(p), dppu<SHL6>(p)};
}
__device__ __forceinline__ float sum8(const LAS unsigned char* p) {
  const f32x4 a = *(const LAS f32x4*)p, b = *(const LAS f32x4*)(p + 16);
  const f32x4 c = a + b;
  return (c[0] + c[1]) + (c[2] + c[3]);
}
__device__ __forceinline__ unsigned arrive(LAS unsigned char* p, int lane) {
  unsigned old = 0;
  CFENCE();
  if (lane == 0) old = __hip_atomic_fetch_add((LAS unsigned*)p, 1u, __ATOMIC_RELAXED, __HIP_MEMORY_SCOPE_WORKGROUP);
  old = __builtin_amdgcn_readfirstlane(old);
  CFENCE();
  return old;
}
__device__ __forceinline__ unsigned lds_word(LAS unsigned char* p) { return __builtin_amdgcn_readfirstlane(*(volatile LAS unsigned*)p); }
#define SLP 8
template <int S = SLP> __device__ __forceinline__ unsigned lds_wait(LAS unsigned char* p, unsigned n) {
  unsigned v;
  while ((v = lds_word(p)) < n) __builtin_amdgcn_s_sleep(S);
  CFENCE();
  return v;
}
template <int NW, bool TAGGED>
__device__ __forceinline__ bool publish64(LAS unsigned char* L, int red, int cnt, const float (&acc)[8], int w, int lane, unsigned tag, rsrc_t rW, int so) {
  ((LAS float*)(L + red))[w * 64 + 8 * (lane & 7) + (lane >> 3)] = reduce8(acc, lane);
  const bool last = arrive(L + cnt, lane) == (unsigned)(NW - 1);
  if (last) {
    if (TAGGED) {
      const LAS f32x2* f = (const LAS f32x2*)(L + red) + (lane & 31);
      const f32x2 s = (f[0] + f[32]) + (f[64] + f[96]);
      if (lane < 32) st128(v4u{f2u(s[0]), tag, f2u(s[1]), tag}, rW, lane * 16, so);
    } else {
      const LAS f32x4* f = (const LAS f32x4*)(L + red) + (lane & 15);
      f32x4 s = (f[0] + f[16]) + (f[32] + f[48]);
      if (NW == 8) s += (f[64] + f[80]) + (f[96] + f[112]);
      if (lane < 16) st128(v4u{nosent(s[0]), nosent(s[1]), nosent(s[2]), nosent(s[3])}, rW, lane * 16, so);
    }
  }
  return last;
}
constexpr int OOB = 0x7FFF0000;
#define GATHER4(g, oa, ob, d, so, RET) do { \
    _Pragma("unroll 1") for (;;) { \
      g[0] = ld128<AUX_SC1>(rW, oa, so); g[1] = ld128<AUX_SC1>(rW, (oa) + (d), so); g[2] = ld128<AUX_SC1>(rW, ob, so); g[3] = ld128<AUX_SC1>(rW, (ob) + (d), so); \
      if (!wave_any(max(max(max4(g[0]), max4(g[1])), max(max4(g[2]), max4(g[3]))) == SENT) || ++spins >= SPIN_CAP) break; \
      __builtin_amdgcn_s_sleep(16); CFENCE(); RETRY(RET); \
    } } while (0)
__device__ __forceinline__ f32x4 asf(v4u v) { return __builtin_bit_cast(f32x4, v); }

#define STAMP(i) do { } while (0)
#define STAMPW(i) do { } while (0)
#define RETRY(i) do { } while (0)

#define FINISH() do { } while (0)

__global__ void __launch_bounds__(832)
fused_kernel(const u16* __restrict__ x, const u16* __restrict__ kc, const u16* __restrict__ vc, const u16* __restrict__ wq,
             const u16* __restrict__ wo, const u16* __restrict__ w1, const u16* __restrict__ b1, const u16* __restrict__ w2,
             const u16* __restrict__ b2, const u16* __restrict__ cnd, const u16* __restrict__ wmod, u16* __restrict__ out,
             unsigned char* ws) {
  __shared__ __attribute__((aligned(16))) unsigned char lds_raw[L_BYTES];
  LAS unsigned char* const L = (LAS unsigned char*)lds_raw;
  const int tid = threadIdx.x, lane = tid & 63, w = __builtin_amdgcn_readfirstlane(tid >> 6);
  const int cb = (blockIdx.x & 7) * 32 + (blockIdx.x >> 3);
  const int grp = cb >> 6, sp = cb & 63, ri = cb >> 1, rj = ri >> 2;
  const bool isX = (cb & 1) == 0;
  const bool isY = !isX && (cb & 31) < 28;
  const int yi = (cb >> 5) * 14 + ((cb & 31) >> 1);
  const bool isRed = isX && (ri & 3) == 0;
  const int r16 = lane & 15, g4 = lane >> 4, rg = lane >> 3;
  unsigned spins = 0;
  v4u pfc;
  {
    unsigned long long pc;
    asm volatile("s_getpc_b64 %0" : "=s"(pc));
    pfc = ld128<0>(mk((const void*)(pc & ~255ull), w == 0 ? CODE_BYTES : 0), lane * 16 + (blockIdx.x >> 3) * 1024, 0);
  }
  asm volatile("" ::"s"(x), "s"(kc), "s"(vc), "s"(wq), "s"(wo), "s"(w1), "s"(b1), "s"(w2), "s"(b2), "s"(cnd), "s"(wmod), "s"(out), "s"(ws));
  const rsrc_t rW = mk(ws, WS_BYTES);
  const int g3o = WS_G3 + ri * 256;

  if (__builtin_expect(w == 8, 1)) {
    int dead = (int)(unsigned)__builtin_amdgcn_s_memrealtime() + TOFF;
    if (lane < 32) ((LAS unsigned*)(L + L_CNT))[lane] = 0u;
    const unsigned lbase = (unsigned)(size_t)L;
    const unsigned pgaddr = lbase + L_PROG + (lane & 7) * 4, fulladdr = lbase + L_FULL;
    int u = 0, pub = 0, lp = 0, pg = 0;
    __builtin_amdgcn_s_setprio(PRL);
    const int uend_all = isY ? U_WEND : U_W;
#pragma unroll 1
    for (int seg = 0; u < uend_all; ++seg) {
      const u16* src; int bytes, stride, so, step, uend, smul = 0, smask = 0;
      if (seg < 2) {
        src = seg ? vc : kc; bytes = 4 * SK * DH * 2; stride = 256; so = cb * 262144; step = 4096;
        uend = seg ? U_W : U_V; smul = seg + 1; smask = 15 - seg;
      } else {
        if (seg == 3) {
          VM(4 * WPRE);
          pub = max(pub, U_W);
          *(volatile LAS int*)(L + L_FULL) = pub;
          lds_wait<1>(L + C_PUB, 8);
        }
        dead += seg == 2 ? (1 << 28) : seg == 3 ? -(1 << 29) : 0;
        src = w2; bytes = FF * D * 2; stride = 2048; step = 256; so = (yi * 32 + (seg == 4 ? 16 : 0)) * 2048 + (seg == 3 ? WPRE * 256 : 0);
        uend = seg == 2 ? U_W + WPRE : seg == 3 ? U_W + 8 : U_WEND;
      }
      const int tau = seg ? TAU2 : TAU2K;
      if (seg == 1) dead += (TAU2K - TAU2) * 32;
      const unsigned long long sa = (unsigned long long)src;
      const v4i srd = {(int)sa, (int)(sa >> 32) & 0xffff, bytes, 0x00020000};
      int vo[4];
#pragma unroll
      for (int i = 0; i < 4; ++i) {
        const int row = 4 * i + g4;
        vo[i] = row * stride + (r16 ^ ((row * smul) & smask)) * 16 - 1024 * i;
      }
      int ustop = seg == 0 ? 4 : uend;
#pragma unroll 1
      for (;;) {
        int t, t2, tv;
        asm volatile(
            "1:\n\t"
            "s_sub_i32 %[t], %[u], 30\n\t"
            "v_cmp_gt_i32 vcc, %[t], %[pg]\n\t"
            "s_add_u32 m0, %[lp], %[b0]\n\t"
            "s_cbranch_vccnz 3f\n\t"
            "buffer_load_dwordx4 %[v0], %[srd], %[so] offen nt lds\n\t"
            "buffer_load_dwordx4 %[v1], %[srd], %[so] offen offset:1024 nt lds\n\t"
            "buffer_load_dwordx4 %[v2], %[srd], %[so] offen offset:2048 nt lds\n\t"
            "buffer_load_dwordx4 %[v3], %[srd], %[so] offen offset:3072 nt lds\n\t"
            "s_add_u32 m0, %[lp], %[b1]\n\t"
            "s_add_u32 %[so], %[so], %[step]\n\t"
            "buffer_load_dwordx4 %[v0], %[srd], %[so] offen nt lds\n\t"
            "buffer_load_dwordx4 %[v1], %[srd], %[so] offen offset:1024 nt lds\n\t"
            "buffer_load_dwordx4 %[v2], %[srd], %[so] offen offset:2048 nt lds\n\t"
            "buffer_load_dwordx4 %[v3], %[srd], %[so] offen offset:3072 nt lds\n\t"
            "ds_read_b32 %[pg], %[pa]\n\t"
            "s_memrealtime vcc\n\t"
            "s_add_u32 %[so], %[so], %[step]\n\t"
            "s_add_u32 %[lp], %[lp], 0x2000\n\t"
            "s_and_b32 %[lp], %[lp], 0x1ffff\n\t"
            "s_add_u32 %[u], %[u], 2\n\t"
            "s_mul_i32 %[t2], %[pub], %[tau]\n\t"
            "s_lshr_b32 %[t2], %[t2], 1\n\t"
            "s_add_u32 %[t2], %[t2], %[dead]\n\t"
            "s_sub_i32 %[t], %[u], %[hu]\n\t"
            "s_waitcnt vmcnt(%[hn]) lgkmcnt(0)\n\t"
            "s_sub_u32 vcc_lo, vcc_lo, %[t2]\n\t"
            "s_cmp_gt_i32 vcc_lo, %[th2]\n\t"
            "s_cbranch_scc1 2f\n\t"
            "s_waitcnt vmcnt(%[mn])\n\t"
            "s_sub_i32 %[t], %[u], %[mu]\n\t"
            "s_cmp_gt_i32 vcc_lo, %[th1]\n\t"
            "s_cbranch_scc1 2f\n\t"
            "s_waitcnt vmcnt(%[ln])\n\t"
            "s_sub_i32 %[t], %[u], %[lu]\n"
            "2:\n\t"
            "s_max_i32 %[pub], %[pub], %[t]\n\t"
            "v_mov_b32 %[tv], %[pub]\n\t"
            "s_cmp_lt_i32 %[u], %[ue]\n\t"
            "ds_write_b32 %[fa], %[tv]\n\t"
            "s_cbranch_scc1 1b\n"
            "3:"
            : [t] "=&s"(t), [tv] "=&v"(tv), [u] "+s"(u), [so] "+s"(so), [lp] "+s"(lp), [t2] "=&s"(t2), [pub] "+s"(pub), [pg] "+v"(pg)
            : [v0] "v"(vo[0]), [v1] "v"(vo[1]), [v2] "v"(vo[2]), [v3] "v"(vo[3]), [srd] "s"(srd), [step] "s"(step), [ue] "s"(ustop), [b0] "s"(lbase),
              [b1] "s"(lbase + 4096), [pa] "v"(pgaddr), [fa] "v"(fulladdr), [tau] "s"(tau), [dead] "s"(dead), [th1] "i"(TH1), [th2] "i"(TH2),
              [hn] "i"(4 * WHI), [hu] "i"(WHI), [mn] "i"(4 * WMID), [mu] "i"(WMID), [ln] "i"(4 * WLO), [lu] "i"(WLO)
            : "memory", "vcc", "scc");
        if (u >= uend) break;
        if (u >= ustop) { wg_barrier(); ustop = uend; continue; }
        pg = ((volatile LAS int*)(L + L_PROG))[lane & 7];
        if (wave_any(pg < u - 30)) {
          VM(0);
          pub = u;
          *(volatile LAS int*)(L + L_FULL) = pub;
          RETRY(27);
          do { __builtin_amdgcn_s_sleep(1); pg = ((volatile LAS int*)(L + L_PROG))[lane & 7]; } while (wave_any(pg < u - 30));
        }
      }
      STAMPW(32 + seg);
    }
#define DRAIN(k) do { VM(4 * (k)); pub = max(pub, uend_all - (k)); *(volatile LAS int*)(L + L_FULL) = pub; } while (0)
    DRAIN(8); DRAIN(4); DRAIN(2); DRAIN(0);
    STAMPW(29);
    __builtin_amdgcn_endpgm();
  }
  if (__builtin_expect(w > 8, 1)) {
    const int s = w - 9, st = tid - 576;
    __builtin_amdgcn_s_setprio(PRS);
    const rsrc_t rC = mk(cnd, D * 2), rX = mk(x, D * 2);
    const int f1i = (cb >> 6) * 16 + (cb & 15), f1k = f1i >> 3, f1c = (f1i & 4) * 4 + ((cb >> 4) & 3) * 4 + (f1i & 3);
    const int g2o = WS_G2 + ((cb & 63) * 4 + (cb >> 6)) * 256;
    v4u f1w[4], f2w[8];
    {
      const rsrc_t rMod = mk(wmod, D * 6 * D * 2);
      const int vo = rg * 12288 + (lane & 7) * 16, so = (f1k * 128 + 32 * s) * 12288 + f1c * 128;
#pragma unroll
      for (int i = 0; i < 4; ++i) f1w[i] = ld128<AUX_NT>(rMod, vo, so + i * 8 * 12288);
    }
    const unsigned c1 = ld16(rC, (lane & 31) * 2, (f1k * 128 + 32 * s) * 2);
    const unsigned epoch = ld32<AUX_SC1>(rW, 0, WS_EPOCH);
    st128(v4u{SENT, SENT, SENT, SENT}, rW, st * 16, WS_SENT + cb * 8192);
    st128(v4u{SENT, SENT, SENT, SENT}, rW, st * 16, WS_SENT + cb * 8192 + 4096);
    st128(v4u{SENT, SENT, SENT, SENT}, rW, st < 16 ? g2o + st * 16 : (st < 32 && isX) ? g3o + (st - 16) * 16 : OOB, 0);
    SB();
    const unsigned c3 = ld32<0>(rC, st * 4, (ri & 1) * 1024);
    const v4u xa = ld128<0>(rX, lane * 32, 0), xb = ld128<0>(rX, lane * 32, 16);
    const unsigned xh = ld32<0>(rX, (lane & 31) * 4, ((cb >> 6) * 256 + 64 * s) * 2);
#define F2LOAD() do { \
      const rsrc_t rQ = mk(wq, D * 4096 * 2); \
      const int vo = rg * 8192 + (lane & 7) * 16, so = ((cb >> 6) * 256 + 64 * s) * 8192 + (cb & 63) * 128; \
      _Pragma("unroll") for (int i = 0; i < 8; ++i) f2w[i] = ld128<AUX_NT>(rQ, vo, so + i * 8 * 8192); \
      SB(); } while (0)
    if (F2POS == 0) F2LOAD();
    wg_barrier();
    LAS float* const ex = (LAS float*)(L + L_F1 + 1024 + s * 256);
    const unsigned tag = epoch + 1u;
    {
      if (F2POS == 1) { VM(8); if (s == 0) STAMPW(40); F2LOAD(); }
      ex[(lane & 7) * 4 + ((lane >> 3) & 3)] = silu_bf(bflo(c1));
      CFENCE();
      const f32x4 sv = *(const LAS f32x4*)(ex + rg * 4);
      float acc[8] = {0, 0, 0, 0, 0, 0, 0, 0};
#pragma unroll
      for (int i = 0; i < 4; ++i) fma8(acc, f1w[i], sv[i]);
      if (s == 0) STAMPW(41);
      VM(F2POS == 2 ? 4 : 12);
      if (s == 0) STAMPW(42);
      if (publish64<4, true>(L, L_F1, C_F1, acc, s, lane, tag, rW, WS_G1 + (f1c * 8 + f1k) * 512)) STAMPW(1);
      if (F2POS == 2) F2LOAD();
    }
    float ln_m, ln_r;
    {
      float v[16];
#pragma unroll
      for (int j = 0; j < 4; ++j) { v[2 * j] = bflo(xa[j]); v[2 * j + 1] = bfhi(xa[j]); v[8 + 2 * j] = bflo(xb[j]); v[9 + 2 * j] = bfhi(xb[j]); }
      float sm = 0.f;
#pragma unroll
      for (int j = 0; j < 16; ++j) sm += v[j];
      const float mean = wave_sum(sm) * (1.f / D);
      float q = 0.f;
#pragma unroll
      for (int j = 0; j < 16; ++j) { const float d = v[j] - mean; q = fmaf(d, d, q); }
      ln_m = rbf(mean); ln_r = ln_rstd(wave_sum(q) * (1.f / D));
    }
    {
      const int g_vo = (lane >> 5) * 65536 + (lane & 31) * 16, g_so = WS_G1 + ((cb >> 6) * 4 + s) * 4096;
      float s0 = 0.f, s1 = 0.f;
      bool bad = true;
      VM(0);
#pragma unroll 1
      for (;;) {
        const int o = bad ? g_vo : OOB;
        v4u g[8];
#pragma unroll
        for (int i = 0; i < 8; ++i) g[i] = ld128<AUX_SC1>(rW, o + i * 512, g_so);
        bool nb = false;
#pragma unroll
        for (int i = 0; i < 8; ++i) nb |= (g[i].y != tag) | (g[i].w != tag);
        const float a0 = ((u2f(g[0].x) + u2f(g[1].x)) + (u2f(g[2].x) + u2f(g[3].x))) + ((u2f(g[4].x) + u2f(g[5].x)) + (u2f(g[6].x) + u2f(g[7].x)));
        const float a1 = ((u2f(g[0].z) + u2f(g[1].z)) + (u2f(g[2].z) + u2f(g[3].z))) + ((u2f(g[4].z) + u2f(g[5].z)) + (u2f(g[6].z) + u2f(g[7].z)));
        s0 = bad ? a0 : s0; s1 = bad ? a1 : s1;
        bad = bad && nb;
        if (!wave_any(bad) || ++spins >= SPIN_CAP) break;
        __builtin_amdgcn_s_sleep(16); CFENCE(); RETRY(22);
      }
      if (s == 0) STAMPW(2);
      STAMPW(43);
      s0 = rbf(s0); s1 = rbf(s1);
      const auto e0s = __builtin_amdgcn_permlane32_swap(f2u(s0), f2u(s0), false, false);
      const auto e1s = __builtin_amdgcn_permlane32_swap(f2u(s1), f2u(s1), false, false);
      const float h0 = rbf(rbf(rbf(rbf(bflo(xh) - ln_m) * ln_r) * rbf(1.f + u2f(e0s[1]))) + u2f(e0s[0]));
      const float h1 = rbf(rbf(rbf(rbf(bfhi(xh) - ln_m) * ln_r) * rbf(1.f + u2f(e1s[1]))) + u2f(e1s[0]));
      LAS float* hp = ex + ((2 * lane) & 7) * 8 + ((lane & 31) >> 2);
      hp[0] = h0; hp[8] = h1;
      CFENCE();
      const f32x4 ha = *(const LAS f32x4*)(ex + rg * 8), hb = *(const LAS f32x4*)(ex + rg * 8 + 4);
      float acc[8] = {0, 0, 0, 0, 0, 0, 0, 0};
#pragma unroll
      for (int i = 0; i < 8; ++i) fma8(acc, f2w[i], i < 4 ? ha[i & 3] : hb[i & 3]);
      if (publish64<4, false>(L, L_F2, C_F2, acc, s, lane, tag, rW, g2o)) STAMPW(3);
    }
    {
      lds_wait(L + L_FULL, GATE_Q);
      lds_wait<1>(L + C_F2, 4);
      const int g_vo = (st >> 4) * 1024 + (st & 15) * 16;
      v4u g[4];
      GATHER4(g, g_vo, g_vo + 512, 256, WS_G2 + grp * 16384, 24);
      const f32x4 q4 = (asf(g[0]) + asf(g[1])) + (asf(g[2]) + asf(g[3]));
      *(LAS v2u*)(L + L_Q + st * 8) = v2u{pk2(q4[0], q4[1]), pk2(q4[2], q4[3])};
      if (arrive(L + C_Q, lane) == 3u) {
        if (cb == 0 && lane == 0) __builtin_amdgcn_raw_buffer_store_b32(tag, rW, 0, WS_EPOCH, AUX_SC1);
        STAMPW(4);
      }
      lds_wait<1>(L + C_Q, 4);
    }
    if (isX) {
      LAS float* const sx = (LAS float*)(L + L_H + s * 512);
#pragma unroll 1
      for (int j = 0; j < 2; ++j) {
        const int r = 2 * lane + j;
        sx[(r >> 5) * 32 + (r & 7) * 4 + ((r >> 3) & 3)] = silu_bf(j ? bfhi(c3) : bflo(c3));
      }
      CFENCE();
      const rsrc_t rMod = mk(wmod, D * 6 * D * 2);
      const int vo = rg * 12288 + (lane & 7) * 16;
      int so = ((ri & 1) * 512 + 128 * s) * 12288 + 4096 + (ri >> 1) * 128;
      float acc[8] = {0, 0, 0, 0, 0, 0, 0, 0};
#pragma unroll 1
      for (int j = 0; j < 2; ++j, so += 64 * 12288) {
        v4u mw[8];
#pragma unroll
        for (int i = 0; i < 8; ++i) mw[i] = ld128<AUX_NT>(rMod, vo, so + i * 8 * 12288);
        SB();
        const f32x4 sv = *(const LAS f32x4*)(sx + j * 64 + rg * 4), sw = *(const LAS f32x4*)(sx + j * 64 + 32 + rg * 4);
#pragma unroll
        for (int i = 0; i < 8; ++i) fma8(acc, mw[i], i < 4 ? sv[i & 3] : sw[i & 3]);
      }
      if (publish64<4, false>(L, L_F3, C_F3, acc, s, lane, 0u, rW, g3o)) STAMPW(40);
    }
    lds_wait(L + L_FULL, GATE_G3);
    if (isY) {
      const int va = 2 * 8192 + (st >> 4) * 512 + (st & 15) * 16, vb = va - 8192;
      v4u g[4];
      GATHER4(g, va, vb, 256, WS_G3, 23);
      const f32x4 cr = asf(g[0]) + asf(g[1]), sr = asf(g[2]) + asf(g[3]);
      const v2u cp = {pk2(1.f + rbf(cr[0]), 1.f + rbf(cr[1])), pk2(1.f + rbf(cr[2]), 1.f + rbf(cr[3]))}, sq = {pk2(sr[0], sr[1]), pk2(sr[2], sr[3])};
      *(LAS v2u*)(L + L_CS + st * 8) = cp;
      *(LAS v2u*)(L + L_CS + 2048 + st * 8) = sq;
      float kc5[5] = {0, 0, 0, 0, 0};
#pragma unroll
      for (int j = 0; j < 4; ++j) {
        const float c = (j & 1) ? bfhi(cp[j >> 1]) : bflo(cp[j >> 1]), sv = (j & 1) ? bfhi(sq[j >> 1]) : bflo(sq[j >> 1]);
        kc5[0] += c; kc5[1] += sv; kc5[2] = fmaf(c, c, kc5[2]); kc5[3] = fmaf(c, sv, kc5[3]); kc5[4] = fmaf(sv, sv, kc5[4]);
      }
#pragma unroll
      for (int i = 0; i < 5; ++i) { const float t = wave_sum(kc5[i]); if (lane == 0) ((LAS float*)(L + L_F1 + 1536))[i * 4 + s] = t; }
      arrive(L + C_CS, lane);
    } else if (isRed && s == 1) {
      const int e = 32 * rj + 4 * (lane & 7), va = ((lane >> 3) & 3) * 8192 + (e >> 6) * 512 + (e & 63) * 4;
      const unsigned xv = ld16(rX, (lane & 31) * 2, rj * 64), bv = ld16(mk(b2, D * 2), (lane & 31) * 2, rj * 64);
      v4u g[4];
      GATHER4(g, va, va, 256, WS_G3, 23);
      const f32x4 m = asf(g[0]) + asf(g[1]);
      const float one = ((lane >> 3) & 3) == 2 ? 1.f : 0.f;
      if (lane < 32) *(LAS f32x4*)(L + L_CS + lane * 16) = f32x4{rbf(one + rbf(m[0])), rbf(one + rbf(m[1])), rbf(one + rbf(m[2])), rbf(one + rbf(m[3]))};
      ((LAS float*)(L + L_CS + 512))[lane] = bflo(lane < 32 ? xv : bv);
      arrive(L + C_CS, lane);
    }
    if (s == 0) {
      if (isX) {
        lds_wait(L + L_FULL, GATE_ST);
        v4u g;
        _Pragma("unroll 1") for (;;) {
          g = ld128<AUX_SC1>(rW, lane * 128, WS_ST + grp * 8192 + ((ri >> 3) & 3) * 16);
          if (!wave_any(max4(g) == SENT) || ++spins >= SPIN_CAP) break;
          __builtin_amdgcn_s_sleep(16); CFENCE(); RETRY(26);
        }
        const float e0 = ex2((u2f(g.x) - wave_max(u2f(g.x))) * LOG2E), e1 = ex2((u2f(g.z) - wave_max(u2f(g.z))) * LOG2E);
        *(LAS f32x2*)(L + L_H + lane * 8) = f32x2{e0 * rcp(wave_sum(e0 * u2f(g.y))), e1 * rcp(wave_sum(e1 * u2f(g.w)))};
        arrive(L + C_ML, lane);
        STAMPW(17);
      }
      if (isY) {
        lds_wait(L + C_CS, 4);
        float kcs[5];
#pragma unroll
        for (int i = 0; i < 5; ++i) { const f32x4 t = *(const LAS f32x4*)(L + L_F1 + 1536 + i * 16); kcs[i] = (t[0] + t[1]) + (t[2] + t[3]); PIN(kcs[i]); }
        lds_wait<4>(L + L_FULL, U_WEND);
        v4u gx;
        _Pragma("unroll 1") do {
          CFENCE();
          gx = ld128<AUX_SC1>(rW, (lane & 31) * XS_STR + (lane >> 5) * 16, WS_XS);
        } while (wave_any(max4(gx) == SENT) && ++spins < SPIN_CAP);
        const float h0 = half_sum(u2f(gx.x)), h1 = half_sum(u2f(gx.y)), h2 = half_sum(u2f(gx.z));
        const auto a0 = __builtin_amdgcn_permlane32_swap(f2u(h0), f2u(h0), false, false);
        const auto a1 = __builtin_amdgcn_permlane32_swap(f2u(h1), f2u(h1), false, false);
        const auto a2 = __builtin_amdgcn_permlane32_swap(f2u(h2), f2u(h2), false, false);
        const float sx = u2f(a0[0]), sxx = u2f(a1[0]), su = u2f(a2[0]), suu = u2f(a0[1]), suc = u2f(a1[1]), sus = u2f(a2[1]);
        const float m1 = sx * (1.f / D);
        const float r1 = ln_rstd(fmaxf(fmaf(sxx, 1.f / D, -m1 * m1), 0.f)), m1b = rbf(m1);
        const float A1 = fmaf(-m1b, kcs[0], su), A2 = fmaf(m1b, fmaf(m1b, kcs[2], -2.f * suc), suu), A3 = fmaf(-m1b, kcs[3], sus);
        const float m2 = fmaf(r1, A1, kcs[1]) * (1.f / D);
        const float e2 = fmaf(r1, fmaf(r1, A2, 2.f * A3), kcs[4]) * (1.f / D);
        const float r2 = ln_rstd(fmaxf(fmaf(-m2, m2, e2), 0.f)), m2b = rbf(m2);
        const float a = r2 * r1;
        if (lane == 0) *(LAS f32x4*)(L + L_ST) = f32x4{a, -a * m1b, r2, -r2 * m2b};
        STAMPW(19);
        wg_barrier();
      }
    }
    __builtin_amdgcn_endpgm();
  }
  {
    __builtin_amdgcn_s_setprio(PRW);
    wg_barrier();
    LAS unsigned char* const prog = L + L_PROG + w * 4;
#define RELEASE(next) do { LGKM0(); *(volatile LAS int*)prog = (next); } while (0)
    const int efrag = 8 * g4 + (lane & 7);
    unsigned k_b;
    v4u wr[8], wf[8];
    const rsrc_t rR = mk(isX ? wo : w1, isX ? 4096 * D * 2 : isY ? D * FF * 2 : 0);
    const int r_vo = isX ? ((g4 >> 1) * 128 + (g4 & 1) * 8) * 2048 + r16 * 16 : g4 * 8 * (FF * 2) + r16 * 4;
    const int r_so = isX ? ((grp * 8 + 2 * ((ri >> 3) & 3)) * 128 + 16 * (ri & 7)) * 2048 + w * 256 : isY ? 128 * w * (FF * 2) + yi * 64 : 0;
#define W1LOAD(i) wr[(i) >> 2][(i) & 3] = ld32<W1AUX>(rR, r_vo, r_so + (((i) >> 3) * 32 + ((i) & 7)) * (FF * 2))
#define RLOAD(m) do { if (isX) wr[m] = ld128<AUX_NT>(rR, r_vo, r_so + (m) * 2048); else { W1LOAD(4 * (m)); W1LOAD(4 * (m) + 1); W1LOAD(4 * (m) + 2); W1LOAD(4 * (m) + 3); } SB(); } while (0)
    k_b = ld16(mk(b1, isY ? FF * 2 : 0), efrag * 2 + yi * 64, 0);
    {
      const int ka = r16 * 256 + ((g4 ^ r16) * 16);
      v4u kp[4][4];
      RLOAD(0);
#pragma unroll
      for (int m = 0; m < 4; ++m) {
        const int u = U_K + 8 * m + w;
        lds_wait(L + L_FULL, u + 1);
#pragma unroll
        for (int ks = 0; ks < 4; ++ks) kp[m][ks] = *(const LAS v4u*)(L + (u & 31) * 4096 + (ka ^ (ks * 64)));
        RELEASE(u + 8);
        if (m == 1) asm volatile("" : "+v"(k_b) : "v"(pfc));
        RLOAD(m + 1);
      }
      STAMP(18);
      lds_wait<2>(L + C_Q, 4);
      v4u qf[4];
#pragma unroll
      for (int ks = 0; ks < 4; ++ks) qf[ks] = *(const LAS v4u*)(L + L_Q + (r16 & 7) * 256 + ks * 64 + g4 * 16);
      const int sw = L_S + (w >> 1) * 512 + (g4 * 8 + (r16 & 7)) * 16 + (w & 1) * 8;
#define QKSTEP(kf, m) do { \
        f32x4 d = {0, 0, 0, 0}; \
        _Pragma("unroll") for (int ks = 0; ks < 4; ++ks) d = mfma(kf[ks], qf[ks], d); \
        const unsigned a = pk2(d[0], d[1]), b = pk2(d[2], d[3]); \
        constexpr float SCALE = 0.08838834764831845f; \
        *(LAS v2u*)(L + sw + (m) * 2048) = v2u{pk2(bflo(a) * SCALE, bfhi(a) * SCALE), pk2(bflo(b) * SCALE, bfhi(b) * SCALE)}; } while (0)
#pragma unroll
      for (int m = 0; m < 4; ++m) QKSTEP(kp[m], m);
#pragma unroll
      for (int m = 4; m < 8; ++m) {
        const int u = U_K + 8 * m + w;
        lds_wait<4>(L + L_FULL, u + 1);
        if (m == 7 && w == 7) STAMPW(52);
        v4u kf[4];
#pragma unroll
        for (int ks = 0; ks < 4; ++ks) kf[ks] = *(const LAS v4u*)(L + (u & 31) * 4096 + (ka ^ (ks * 64)));
        RELEASE(m == 7 ? U_V : u + 8);
        if (m < 7) RLOAD(m + 1);
        QKSTEP(kf, m);
      }
    }
    {
      LAS unsigned char* const sp_ = L + L_S + ((lane >> 5) * 4 + (w >> 1)) * 512 + (lane & 31) * 16 + (w & 1) * 8;
      CFENCE();
      float sc[16];
      float mx = -INFINITY;
#pragma unroll
      for (int i = 0; i < 4; ++i) {
        const v2u s = *(const LAS v2u*)(sp_ + i * 4096);
        sc[4 * i] = bflo(s.x); sc[4 * i + 1] = bfhi(s.x); sc[4 * i + 2] = bflo(s.y); sc[4 * i + 3] = bfhi(s.y);
      }
#pragma unroll
      for (int i = 0; i < 16; ++i) mx = fmaxf(mx, sc[i]);
      mx = max_hi3(mx);
      if (lane < 8) ((LAS float*)(L + L_ST))[w * 8 + lane] = mx;
      if (w == 7) STAMPW(53);
      arrive(L + C_ST, lane);
      lds_wait<SLPC>(L + C_ST, 8);
      if (w == 7) STAMPW(54);
      if (w == 0) STAMPW(55);
      const float M = max_hi3(((const LAS float*)(L + L_ST))[lane]);
      float l = 0.f;
#pragma unroll
      for (int i = 0; i < 4; ++i) {
        float p[4];
#pragma unroll
        for (int j = 0; j < 4; ++j) { p[j] = ex2((sc[4 * i + j] - M) * LOG2E); l += p[j]; }
        *(LAS v2u*)(sp_ + i * 4096) = v2u{pk2(p[0], p[1]), pk2(p[2], p[3])};
      }
      l = sum_hi3(l);
      if (w == 7) STAMPW(56);
      if (w == 0) STAMPW(57);
      if (lane < 8) ((LAS float*)(L + L_ST + 256))[w * 8 + lane] = l;
      if (arrive(L + C_P, lane) == 7u) {
        const float Ls = sum_hi3(((const LAS float*)(L + L_ST + 256))[lane]);
        const v4u o = {nosent(M), nosent(Ls), nosent(dppf<SHL1>(M)), nosent(dppf<SHL1>(Ls))};
        if (lane < 8 && !(lane & 1)) st128(o, rW, lane * 8, WS_ST + cb * 128);
        STAMPW(5);
      }
    }
    if (isX) {
#pragma unroll
      for (int jd = 0; jd < 8; ++jd) wf[jd] = tr8(wr, jd);
    } else {
#pragma unroll
      for (int ks = 0; ks < 4; ++ks)
#pragma unroll
        for (int u = 0; u < 4; ++u) {
          const unsigned lo = wr[2 * ks + (u >> 1)][(2 * u) & 3], hi = wr[2 * ks + (u >> 1)][(2 * u + 1) & 3];
          wf[2 * ks][u] = __builtin_amdgcn_perm(hi, lo, 0x05040100u); wf[2 * ks + 1][u] = __builtin_amdgcn_perm(hi, lo, 0x07060302u);
        }
    }
    float k_w = 0.f, k_ga = 0.f, k_gb = 0.f, k_c = 0.f, k_s = 0.f, k_x = 0.f, k_b2 = 0.f;
    {
      const int vrow = 4 * g4 + (r16 >> 2);
      const int va = vrow * 256 + (((2 * w + ((lane >> 1) & 1)) ^ ((vrow & 7) << 1)) * 16) + (lane & 1) * 8;
      const int pa = L_S + (g4 * 8 + (r16 & 7)) * 16;
      unsigned full = 0;
#define VREAD(dst, t, S) do { \
        const int u_ = U_V + 2 * (t); \
        if (full < (unsigned)(u_ + 2)) full = lds_wait<S>(L + L_FULL, u_ + 2); \
        LAS unsigned char* p_ = L + (u_ & 31) * 4096 + va; \
        const v2u v0_ = __builtin_bit_cast(v2u, __builtin_amdgcn_ds_read_tr16_b64_v4i16((LAS v4s*)p_)); \
        const v2u v1_ = __builtin_bit_cast(v2u, __builtin_amdgcn_ds_read_tr16_b64_v4i16((LAS v4s*)(p_ + 4096))); \
        dst = v4u{v0_.x, v0_.y, v1_.x, v1_.y}; } while (0)
      lds_wait<1>(L + C_P, 8);
      STAMP(6);
      f32x4 acc = {0, 0, 0, 0};
#pragma unroll 1
      for (int t = 0; t < 28; t += 4) {
        v4u pf[4], vf[4];
#pragma unroll
        for (int i = 0; i < 4; ++i) pf[i] = *(const LAS v4u*)(L + pa + (t + i) * 512);
        VREAD(vf[3], t + 3, 4);
#pragma unroll
        for (int i = 0; i < 3; ++i) VREAD(vf[i], t + i, 2);
#pragma unroll
        for (int i = 0; i < 4; ++i) acc = mfma(vf[i], pf[i], acc);
        RELEASE(U_V + 2 * t + 8);
      }
#pragma unroll 1
      for (int t = 28; t < 32; ++t) {
        const v4u pfr = *(const LAS v4u*)(L + pa + t * 512);
        v4u vf;
        VREAD(vf, t, 1);
        acc = mfma(vf, pfr, acc);
      }
      if (r16 < 8)
        st128(v4u{nosent(acc[0]), nosent(acc[1]), nosent(acc[2]), nosent(acc[3])}, rW, (r16 >> 1) * 8 * STR_A + (r16 & 1) * 64 + g4 * 16,
              WS_PA + (grp * 32 + w) * STR_A + sp * 128);
      SB();
      if (isY) arrive(L + C_PUB, lane);
    }
    STAMPW(7);
    const int ecomp = 4 * (lane & 7) + (g4 & 1) * 2 + (g4 >> 1);
    if (isX || isY) {
      float kC = 0.f, kZ = 0.f, kO = 0.f;
      if (isX) {
        lds_wait<1>(L + C_ML, 1);
        k_w = ((const LAS float*)(L + L_H))[(tid >> 3) * 2 + ((tid >> 2) & 1)];
        if (isRed && w < 2) {
          lds_wait<1>(L + C_CS, 1);
          const LAS float* kp = (const LAS float*)(L + L_CS) + (lane & 31);
          k_ga = kp[0]; k_s = kp[32]; k_c = kp[64]; k_gb = kp[96]; k_x = kp[128]; k_b2 = kp[160];
        }
        v4u g;
        _Pragma("unroll 1") do {
          CFENCE();
          g = ld128<AUX_SC1>(rW, tid * 16, WS_PA + ri * STR_A);
        } while (wave_any(max4(g) == SENT) && ++spins < SPIN_CAP);
        const float part = reduce4(asf(g) * k_w);
        if (!(lane & 8)) ((LAS float*)(L + L_F1))[ecomp * 12 + w] = part;
      }
      int yrole = cb & 1;
      asm volatile("" : "+s"(yrole));
      if (yrole) {
        {
        lds_wait(L + C_CS, 4);
        f32x4 h0 = {0, 0, 0, 0}, h1 = {0, 0, 0, 0};
#pragma unroll
        for (int ks = 0; ks < 4; ++ks) {
          v4u a = *(const LAS v4u*)(L + L_CS + (r16 & 1) * 2048 + w * 256 + ks * 64 + g4 * 16);
          if (r16 & 2) a = v4u{0x3F803F80u, 0x3F803F80u, 0x3F803F80u, 0x3F803F80u};
          h0 = mfma(a, wf[2 * ks], h0); h1 = mfma(a, wf[2 * ks + 1], h1);
        }
        if (lane < 16) {
          LAS float* p = (LAS float*)(L + L_F2) + lane * 56 + w;
          p[0] = h0[0]; p[8] = h0[1]; p[16] = h0[2]; p[28] = h1[0]; p[36] = h1[1]; p[44] = h1[2];
        }
        arrive(L + C_PRE, lane);
        }
        lds_wait<1>(L + C_PRE, 8);
        kC = sum8(L + L_F2 + efrag * 112); kZ = sum8(L + L_F2 + efrag * 112 + 32); kO = sum8(L + L_F2 + efrag * 112 + 64);
        PIN(kC); PIN(kZ); PIN(kO);
        v4u f1[8];
#pragma unroll
        for (int i = 0; i < 8; ++i)
#pragma unroll
          for (int u = 0; u < 4; ++u) asm volatile("v_mov_b32 %0, %1" : "=&v"(f1[i][u]) : "v"(wf[i][u]));
        lds_wait<2>(L + L_FULL, U_W + 8 + w + 1);
        {
          v4u w2r[8];
#pragma unroll
          for (int i = 0; i < 8; ++i) w2r[i] = *(const LAS v4u*)(L + ((U_W + 8 * (g4 >> 1) + w) & 31) * 4096 + (g4 & 1) * 2048 + r16 * 16 + i * 256);
#pragma unroll
          for (int jd = 0; jd < 8; ++jd) { wf[jd] = tr8(w2r, jd); PIN(wf[jd]); }
        }
        lds_wait<2>(L + L_FULL, U_WEND);
        STAMP(16);
        v4u ua[4];
        _Pragma("unroll 1") do {
          CFENCE();
#pragma unroll
          for (int ks = 0; ks < 4; ++ks) ua[ks] = ld128<AUX_SC1>(rW, (r16 & 1) * 64 + g4 * 16 + ks * 128, WS_U + w * 512);
        } while (wave_any(max(max(max4(ua[0]), max4(ua[1])), max(max4(ua[2]), max4(ua[3]))) == SENT) && ++spins < SPIN_CAP);
        STAMP(12);
        f32x4 h0 = {0, 0, 0, 0}, h1 = {0, 0, 0, 0};
#pragma unroll
        for (int ks = 0; ks < 4; ++ks) { h0 = mfma(ua[ks], f1[2 * ks], h0); h1 = mfma(ua[ks], f1[2 * ks + 1], h1); }
        if (lane < 16) { ((LAS float*)(L + L_F1))[lane * 24 + w] = h0[0] + h0[1]; ((LAS float*)(L + L_F1))[lane * 24 + 12 + w] = h1[0] + h1[1]; }
      }
      wg_barrier();
      STAMP(isX ? 8 : 21);
      {
        float v = sum8(L + L_F1 + efrag * 48);
        if (!isX) {
          const f32x4 k = *(const LAS f32x4*)(L + L_ST);
          const float t = rbf(rbf(fmaf(k[0], v, fmaf(k[1], kC, fmaf(k[2], kZ, k[3] * kO)))) + bflo(k_b));
          const float z2 = (-2.f * 0.7978845608028654f * LOG2E) * fmaf(0.044715f * t * t, t, t);
          v = t * rcp(1.f + ex2(z2));
        }
        const v4u af = frag_from_lanes(v);
        f32x4 d[8];
#pragma unroll
        for (int jd = 0; jd < 8; ++jd) d[jd] = mfma(af, wf[jd], f32x4{0, 0, 0, 0});
        const bool hi = lane >= 32;
        const v4u o4 = {nosent(hi ? d[4][0] : d[0][0]), nosent(hi ? d[5][0] : d[1][0]), nosent(hi ? d[6][0] : d[2][0]), nosent(hi ? d[7][0] : d[3][0])};
        if (!(g4 & 1)) st128(o4, rW, (r16 >> 2) * STR_B + (r16 & 3) * 32 + (g4 >> 1) * 16, (isX ? WS_PB + ri * 128 : WS_PD + yi * 128) + 4 * w * STR_B);
      }
      STAMP(isX ? 9 : 13);
      if (isRed) {
        float x2 = 0.f;
#pragma unroll 1
        for (int pass = 0; pass < 2; ++pass) {
          const rsrc_t rP = mk(ws + (pass ? WS_PD : WS_PB) + rj * STR_B, pass ? 14336 : 16384);
          v4u ga, gb;
          _Pragma("unroll 1") do {
            CFENCE();
            ga = ld128<AUX_SC1>(rP, tid * 16, 0); gb = ld128<AUX_SC1>(rP, tid * 16 + 8192, 0);
          } while (wave_any(max(max4(ga), max4(gb)) == SENT) && ++spins < SPIN_CAP);
          const float p2 = reduce4(asf(ga) + asf(gb));
          LAS unsigned char* const tp = L + L_F2 + pass * 2048;
          if (!(lane & 8)) ((LAS float*)tp)[ecomp * 12 + w] = p2;
          wg_barrier();
          STAMP(pass ? 14 : 10);
          if (w < 2) {
            const float sm = rbf(sum8(tp + (lane & 31) * 48));
            const bool hi = lane >= 32;
            if (pass == 0) {
              x2 = rbf(k_x + rbf(k_ga * sm));
              const float uu = x2 * k_c;
              if (w == 0) {
                const v4u f = frag_from_lanes(hi ? uu - rbf(uu) : uu);
                if (!(lane & 7)) st128(v4u{nosentu(f.x), nosentu(f.y), nosentu(f.z), nosentu(f.w)}, rW, lane * 2, WS_U + rj * 128);
              } else {
                const float s0 = half_sum(hi ? uu * uu : x2), s1 = half_sum(hi ? uu * k_c : x2 * x2), s2 = half_sum(hi ? uu * k_s : uu);
                if (!(lane & 31)) st128(v4u{nosent(s0), nosent(s1), nosent(s2), 0u}, rW, (lane >> 5) * 16, WS_XS + rj * XS_STR);
              }
            } else if (w == 0) {
              const v4u f = frag_from_lanes(x2 + rbf(k_gb * rbf(sm + k_b2)));
              if (!hi && !(lane & 7)) *(v4u*)(out + rj * 32 + lane) = f;
            }
          }
          STAMP(pass ? 15 : 11);
        }
      }
    }
    FINISH();
  }
}

}

extern "C" void kernel_launch(void* const* d_in, const int* in_sizes, int n_in, void* d_out, int out_size, void* d_ws, size_t ws_size,
                              hipStream_t stream) {
  (void)in_sizes; (void)n_in; (void)out_size; (void)ws_size;
  fused_kernel<<<dim3(256), dim3(832), 0, stream>>>(
      (const u16*)d_in[0], (const u16*)d_in[1], (const u16*)d_in[2], (const u16*)d_in[3], (const u16*)d_in[4], (const u16*)d_in[6],
      (const u16*)d_in[7], (const u16*)d_in[8], (const u16*)d_in[9], (const u16*)d_in[10], (const u16*)d_in[11], (u16*)d_out,
      (unsigned char*)d_ws);
}
```

```cpp
#include <hip/hip_runtime.h>
#include <hip/hip_bf16.h>
#include <stdint.h>

typedef unsigned short u16;
typedef unsigned v4u __attribute__((ext_vector_type(4)));
typedef unsigned v2u __attribute__((ext_vector_type(2)));
typedef int v4i __attribute__((ext_vector_type(4)));
typedef short v4s __attribute__((ext_vector_type(4)));
typedef float f32x4 __attribute__((ext_vector_type(4)));
typedef float f32x2 __attribute__((ext_vector_type(2)));
typedef short bf16x8 __attribute__((ext_vector_type(8)));
typedef __bf16 bf16x2_t __attribute__((ext_vector_type(2)));
typedef __amdgpu_buffer_rsrc_t rsrc_t;
#define LAS __attribute__((address_space(3)))

namespace {

constexpr int D = 1024, SK = 65536, DH = 128, FF = 3584;
constexpr int AUX_NT = 2, AUX_SC1 = 16;
constexpr unsigned SENT = 0xFFFFFFFFu;
constexpr int CODE_BYTES = 20 * 1024;
constexpr unsigned SPIN_CAP = 1u << 17;
constexpr float LOG2E = 1.4426950408889634f;

#define TAU2 27
#define TH1 -30
#define TH2 20
#define TH3 70
#define W0 4
#define W1 5
#define W2 9
#define W3 13
#define TOFF 460

constexpr int WS_EPOCH = 0;
constexpr int WS_G1 = 4096;
constexpr int WS_G2 = WS_G1 + 131072;
constexpr int WS_G3 = WS_G2 + 65536;
constexpr int WS_SENT = 512 * 1024;
constexpr int STR_A = 8192 + 128, STR_B = 16384 + 14336 + 128;
constexpr int WS_PA = WS_SENT;
constexpr int WS_PB = WS_PA + 128 * STR_A;
constexpr int WS_PD = WS_PB + 16384;
constexpr int WS_ST = WS_PB + 32 * STR_B;
constexpr int WS_SENT_END = WS_ST + 32768;
static_assert(WS_SENT_END <= WS_SENT + 2 * 1024 * 1024, "sentinel region");
constexpr int WS_U = WS_G3 + 65536, U_REP = 8192;
static_assert(WS_U + 8 * U_REP <= WS_SENT, "replicas");
constexpr int WS_DIAG = WS_SENT + 2 * 1024 * 1024;
constexpr int WS_BYTES = WS_DIAG + 256 * 64 * 8;

constexpr int U_K = 0, U_V = 64, U_W = 128, U_WEND = 144;
#define TAU2K 37
#define GATE_Q 34
#define GATE_G3 (U_V + 24)
#define GATE_ST (U_V + 40)
#define F2POS 2
#define PRL 0
#define PRS 3
#define PRW 2
#define W1AUX 0
#define SLPC 1
#define UINIT 4
#define VTOUCH 1
#define WPRE 4

constexpr int L_CNT = 0;
constexpr int C_F1 = 0, C_F2 = 4, C_F3 = 8, C_Q = 12, C_ST = 16, C_CS = 20, C_P = 24, C_ML = 28, C_PUB = 36, C_PRE = 40, L_FULL = 48, L_PROG = 64;
constexpr int L_ST = 128;
constexpr int L_Q = L_ST + 512;
constexpr int L_F1 = L_Q + 2048;
constexpr int L_F2 = L_F1 + 2048;
constexpr int L_F3 = L_F2 + 2048;
constexpr int L_H = L_F3 + 2048;
constexpr int L_CS = L_H + 2048;
constexpr int L_DIAG = L_CS + 4096;
#define DIAG_RF 15192
static_assert(DIAG_RF == L_DIAG + 27 * 8, "diag");
constexpr int L_S = 16384;
constexpr int L_RING = 32768;
constexpr int L_BYTES = L_RING + 131072;
static_assert(L_DIAG + 512 <= L_S && L_RING == 32768 && L_FULL == 48, "LDS map (the loader loop has these literally)");

__device__ __forceinline__ float u2f(unsigned u) { return __builtin_bit_cast(float, u); }
__device__ __forceinline__ unsigned f2u(float f) { return __builtin_bit_cast(unsigned, f); }
__device__ __forceinline__ float bflo(unsigned p) { return u2f(p << 16); }
__device__ __forceinline__ float bfhi(unsigned p) { return u2f(p & 0xffff0000u); }
__device__ __forceinline__ unsigned pk2(float lo, float hi) {
  f32x2 v = {lo, hi};
  return __builtin_bit_cast(unsigned, __builtin_convertvector(v, bf16x2_t));
}
__device__ __forceinline__ float rbf(float f) { return bflo(pk2(f, f)); }
template <int CTRL> __device__ __forceinline__ unsigned dppu(unsigned x) { return (unsigned)__builtin_amdgcn_mov_dpp((int)x, CTRL, 0xf, 0xf, true); }
template <int CTRL> __device__ __forceinline__ float dppf(float x) { return u2f(dppu<CTRL>(f2u(x))); }
constexpr int XOR1 = 0xB1, XOR2 = 0x4E, XOR7 = 0x141, ROR8 = 0x128, SHL1 = 0x101, SHL2 = 0x102, SHL4 = 0x104, SHL6 = 0x106;
__device__ __forceinline__ float sum_sw32(float a, float b) { auto r = __builtin_amdgcn_permlane32_swap(f2u(a), f2u(b), false, false); return u2f(r[0]) + u2f(r[1]); }
__device__ __forceinline__ float sum_sw16(float a, float b) { auto r = __builtin_amdgcn_permlane16_swap(f2u(a), f2u(b), false, false); return u2f(r[0]) + u2f(r[1]); }
__device__ __forceinline__ float max_sw32(float a) { auto r = __builtin_amdgcn_permlane32_swap(f2u(a), f2u(a), false, false); return fmaxf(u2f(r[0]), u2f(r[1])); }
__device__ __forceinline__ float max_sw16(float a) { auto r = __builtin_amdgcn_permlane16_swap(f2u(a), f2u(a), false, false); return fmaxf(u2f(r[0]), u2f(r[1])); }
__device__ __forceinline__ float half_sum(float v) {
  v += dppf<XOR1>(v); v += dppf<XOR2>(v); v += dppf<XOR7>(v); v += dppf<ROR8>(v);
  return sum_sw16(v, v);
}
__device__ __forceinline__ float wave_sum(float v) { v = half_sum(v); return sum_sw32(v, v); }
__device__ __forceinline__ float max_hi3(float v) { v = fmaxf(v, dppf<ROR8>(v)); return max_sw32(max_sw16(v)); }
__device__ __forceinline__ float wave_max(float v) { v = fmaxf(v, dppf<XOR1>(v)); v = fmaxf(v, dppf<XOR2>(v)); v = fmaxf(v, dppf<XOR7>(v)); return max_hi3(v); }
__device__ __forceinline__ float sum_hi3(float v) { v += dppf<ROR8>(v); v = sum_sw16(v, v); return sum_sw32(v, v); }
__device__ __forceinline__ f32x4 mfma(v4u a, v4u b, f32x4 c) {
  return __builtin_amdgcn_mfma_f32_16x16x32_bf16(__builtin_bit_cast(bf16x8, a), __builtin_bit_cast(bf16x8, b), c, 0, 0, 0);
}
__device__ __forceinline__ rsrc_t mk(const void* p, int bytes) { return __builtin_amdgcn_make_buffer_rsrc((void*)p, 0, bytes, 0x00020000); }
template <int AUX> __device__ __forceinline__ v4u ld128(rsrc_t r, int vo, int so) { return __builtin_amdgcn_raw_buffer_load_b128(r, vo, so, AUX); }
template <int AUX> __device__ __forceinline__ v2u ld64(rsrc_t r, int vo, int so) { return __builtin_amdgcn_raw_buffer_load_b64(r, vo, so, AUX); }
template <int AUX> __device__ __forceinline__ unsigned ld32(rsrc_t r, int vo, int so) { return __builtin_amdgcn_raw_buffer_load_b32(r, vo, so, AUX); }
__device__ __forceinline__ unsigned ld16(rsrc_t r, int vo, int so) { return __builtin_amdgcn_raw_buffer_load_b16(r, vo, so, 0); }
__device__ __forceinline__ void st128(v4u v, rsrc_t r, int vo, int so) { __builtin_amdgcn_raw_buffer_store_b128(v, r, vo, so, AUX_SC1); }
__device__ __forceinline__ void st64(v2u v, rsrc_t r, int vo, int so) { __builtin_amdgcn_raw_buffer_store_b64(v, r, vo, so, AUX_SC1); }
#define STR_(x) #x
#define STR(x) STR_(x)
#define SB() __builtin_amdgcn_sched_barrier(0)
#define CFENCE() asm volatile("" ::: "memory")
#define LGKM0() asm volatile("s_waitcnt lgkmcnt(0)" ::: "memory")
#define VM(N) asm volatile("s_waitcnt vmcnt(%0)" ::"i"(N) : "memory")
#define PIN(a) asm volatile("" : "+v"(a))
__device__ __forceinline__ void wg_barrier() {
  LGKM0(); SB();
  __builtin_amdgcn_s_barrier();
  CFENCE(); SB();
}
__device__ __forceinline__ unsigned nosentu(unsigned v) { return min(v, 0xFFFFFFFEu); }
__device__ __forceinline__ unsigned nosent(float v) { return nosentu(f2u(v)); }
__device__ __forceinline__ unsigned max4(v4u v) { return max(max(v.x, v.y), max(v.z, v.w)); }
__device__ __forceinline__ bool wave_any(bool p) { return __builtin_amdgcn_ballot_w64(p) != 0ull; }
__device__ __forceinline__ float rcp(float x) { return __builtin_amdgcn_rcpf(x); }
__device__ __forceinline__ float ex2(float x) { return __builtin_amdgcn_exp2f(x); }
__device__ __forceinline__ float silu_bf(float x) { return rbf(x / (1.f + expf(-x))); }
__device__ __forceinline__ float ln_rstd(float var) { return rcp(rbf(__builtin_amdgcn_sqrtf(rbf(rbf(var) + 1e-5f)))); }

__device__ __forceinline__ void fma8(float (&acc)[8], v4u w, float x) {
#pragma unroll
  for (int j = 0; j < 4; ++j) { acc[2 * j] = fmaf(x, bflo(w[j]), acc[2 * j]); acc[2 * j + 1] = fmaf(x, bfhi(w[j]), acc[2 * j + 1]); }
}
__device__ __forceinline__ float reduce8(const float (&a)[8], int lane) {
  float b0 = sum_sw32(a[0], a[4]), b1 = sum_sw32(a[1], a[5]), b2 = sum_sw32(a[2], a[6]), b3 = sum_sw32(a[3], a[7]);
  float c0 = sum_sw16(b0, b2), c1 = sum_sw16(b1, b3);
  const bool b = lane & 8;
  float keep = b ? c1 : c0, send = b ? c0 : c1;
  return keep + dppf<ROR8>(send);
}
__device__ __forceinline__ float reduce4(f32x4 v) {
  float a = sum_sw32(v[0], v[1]), b = sum_sw32(v[2], v[3]);
  float c = sum_sw16(a, b);
  return c + dppf<ROR8>(c);
}
__device__ __forceinline__ v4u tr8(const v4u* in, int jd) {
  v4u o;
#pragma unroll
  for (int u = 0; u < 4; ++u) o[u] = __builtin_amdgcn_perm(in[2 * u + 1][jd >> 1], in[2 * u][jd >> 1], (jd & 1) ? 0x07060302u : 0x05040100u);
  return o;
}
__device__ __forceinline__ v4u frag_from_lanes(float v) {
  unsigned p = pk2(v, dppf<SHL1>(v));
  return v4u{p, dppu<SHL2>(p), dppu<SHL4>(p), dppu<SHL6>(p)};
}
__device__ __forceinline__ float sum8(const LAS unsigned char* p) {
  const f32x4 a = *(const LAS f32x4*)p, b = *(const LAS f32x4*)(p + 16);
  const f32x4 c = a + b;
  return (c[0] + c[1]) + (c[2] + c[3]);
}
template <int OFF> __device__ __forceinline__ void arrive(unsigned zv) {
  unsigned one;
  asm volatile("v_mov_b32 %0, 1\n\ts_mov_b64 exec, 1\n\tds_add_u32 %1, %0 offset:%2\n\ts_mov_b64 exec, -1" : "=&v"(one) : "v"(zv), "i"(OFF) : "memory");
}
template <int OFF> __device__ __forceinline__ unsigned arrive_rtn(unsigned zv) {
  unsigned t, old;
  asm volatile("v_mov_b32 %0, 1\n\ts_mov_b64 exec, 1\n\tds_add_rtn_u32 %0, %2, %0 offset:%3\n\ts_mov_b64 exec, -1\n\ts_waitcnt lgkmcnt(0)\n\tv_readfirstlane_b32 %1, %0"
               : "=&v"(t), "=s"(old) : "v"(zv), "i"(OFF) : "memory");
  return old;
}
template <int OFF> __device__ __forceinline__ void lds_post(unsigned av, int sval) {
  unsigned t;
  asm volatile("v_mov_b32 %0, %2\n\tds_write_b32 %1, %0 offset:%3" : "=&v"(t) : "v"(av), "s"(sval), "i"(OFF) : "memory");
}
#define SLP 8
template <int OFF, int S = SLP> __device__ __forceinline__ unsigned lds_wait(unsigned zv, unsigned n) {
  unsigned t, v;
  asm volatile("s_branch 2f\n1:\n\ts_sleep %5\n2:\n\tds_read_b32 %0, %2 offset:%4\n\ts_waitcnt lgkmcnt(0)\n\tv_readfirstlane_b32 %1, %0\n\ts_cmp_lt_u32 %1, %3\n\ts_cbranch_scc1 1b"
               : "=&v"(t), "=&s"(v) : "v"(zv), "s"(n), "i"(OFF), "i"(S) : "memory", "scc");
  return v;
}
template <int CNT, bool TAGGED>
__device__ __forceinline__ bool publish64(LAS unsigned char* L, unsigned zv, int red, const float (&acc)[8], int w, int lane, unsigned tag, rsrc_t rW, int so) {
  ((LAS float*)(L + red))[w * 64 + 8 * (lane & 7) + (lane >> 3)] = reduce8(acc, lane);
  const bool last = arrive_rtn<CNT>(zv) == 3u;
  if (last) {
    if (TAGGED) {
      const LAS f32x2* f = (const LAS f32x2*)(L + red) + (lane & 31);
      const f32x2 s = (f[0] + f[32]) + (f[64] + f[96]);
      if (lane < 32) st128(v4u{f2u(s[0]), tag, f2u(s[1]), tag}, rW, lane * 16, so);
    } else {
      const LAS f32x4* f = (const LAS f32x4*)(L + red) + (lane & 15);
      const f32x4 s = (f[0] + f[16]) + (f[32] + f[48]);
      if (lane < 16) st128(v4u{nosent(s[0]), nosent(s[1]), nosent(s[2]), nosent(s[3])}, rW, lane * 16, so);
    }
  }
  return last;
}
constexpr int OOB = 0x7FFF0000;
#define GATHER4(g, oa, ob, d, so, RET) do { \
    _Pragma("unroll 1") for (;;) { \
      g[0] = ld128<AUX_SC1>(rW, oa, so); g[1] = ld128<AUX_SC1>(rW, (oa) + (d), so); g[2] = ld128<AUX_SC1>(rW, ob, so); g[3] = ld128<AUX_SC1>(rW, (ob) + (d), so); \
      if (!wave_any(max(max(max4(g[0]), max4(g[1])), max(max4(g[2]), max4(g[3]))) == SENT) || ++spins >= SPIN_CAP) break; \
      __builtin_amdgcn_s_sleep(16); CFENCE(); RETRY(RET); \
    } } while (0)
__device__ __forceinline__ int kvswz(int row) { return ((row & 7) << 1) | (row >> 3); }
__device__ __forceinline__ f32x4 asf(v4u v) { return __builtin_bit_cast(f32x4, v); }

#define STAMP(i) do { } while (0)
#define STAMPW(i) do { } while (0)
#define RETRY(i) do { } while (0)

#define RINGFULL
#define FINISH() do { } while (0)

__global__ void __launch_bounds__(832)
fused_kernel(const u16* __restrict__ x, const u16* __restrict__ kc, const u16* __restrict__ vc, const u16* __restrict__ wq,
             const u16* __restrict__ wo, const u16* __restrict__ w1, const u16* __restrict__ b1, const u16* __restrict__ w2,
             const u16* __restrict__ b2, const u16* __restrict__ cnd, const u16* __restrict__ wmod, u16* __restrict__ out,
             unsigned char* ws) {
  __shared__ __attribute__((aligned(16))) unsigned char lds_raw[L_BYTES];
  LAS unsigned char* const L = (LAS unsigned char*)lds_raw;
  const int tid = threadIdx.x, lane = tid & 63, w = __builtin_amdgcn_readfirstlane(tid >> 6);
  const int cb = (blockIdx.x & 7) * 32 + (blockIdx.x >> 3);
  const int grp = cb >> 6, sp = cb & 63, ri = cb >> 1, rj = ri >> 2;
  const bool isX = (cb & 1) == 0;
  const bool isY = !isX && (cb & 31) < 28;
  const int yi = (cb >> 5) * 14 + ((cb & 31) >> 1);
  const bool isRed = isX && (ri & 3) == 0;
  const int r16 = lane & 15, g4 = lane >> 4, rg = lane >> 3;
  unsigned spins = 0, zv = 0;
  v4u pfc;
  {
    const unsigned long long pc = __builtin_amdgcn_s_getpc();
    pfc = ld128<0>(mk((const void*)(pc & ~255ull), w == 0 ? CODE_BYTES : 0), lane * 16 + (blockIdx.x >> 3) * 1024, 0);
  }
  asm volatile("" ::"s"(x), "s"(kc), "s"(vc), "s"(wq), "s"(wo), "s"(w1), "s"(b1), "s"(w2), "s"(b2), "s"(cnd), "s"(wmod), "s"(out), "s"(ws));
  PIN(zv);
  const rsrc_t rW = mk(ws, WS_BYTES);
  const int g3o = WS_G3 + ri * 256;

  if (__builtin_expect(w == 8, 1)) {
    int vo[4];
#pragma unroll
    for (int i = 0; i < 4; ++i) vo[i] = g4 * 256 + (r16 ^ kvswz(4 * i + g4)) * 16;
    int dead = (int)(unsigned)__builtin_amdgcn_s_memrealtime() + TOFF - (1 << 28);
    if (lane < 32) ((LAS unsigned*)(L + L_CNT))[lane] = 0u;
    const unsigned pgaddr = L_PROG + (lane & 7) * 4;
    int u = 0, pub = 0, lp = 0, pg = (int)zv;
    asm volatile("" : "+s"(u), "+s"(pub), "+s"(lp), "+s"(dead));
    auto run = [&](const u16* src, int bytes, int& so, int step, int uend, int tau) __attribute__((always_inline)) {
      const unsigned long long sa = (unsigned long long)src;
      const v4i srd = {(int)sa, (int)(sa >> 32) & 0xffff, bytes, 0x00020000};
      {
        int t, t2, tv;
        asm volatile(
            "s_branch 1f\n"
            "3:\n\t"
            "ds_read_b32 %[pg], %[pa]\n\t"
            "s_waitcnt lgkmcnt(0)\n\t"
            "v_cmp_gt_i32 vcc, %[t], %[pg]\n\t"
            "s_cbranch_vccz 1f\n\t"
            "s_waitcnt vmcnt(0)\n\t"
            "s_mov_b32 %[pub], %[u]\n\t"
            "v_mov_b32 %[tv], %[pub]\n\t"
            "ds_write_b32 %[zv], %[tv] offset:48\n\t"
            "s_sleep 1\n\t"
            RINGFULL
            "s_branch 3b\n"
            "1:\n\t"
            "s_sub_i32 %[t], %[u], 30\n\t"
            "v_cmp_gt_i32 vcc, %[t], %[pg]\n\t"
            "s_add_u32 m0, %[lp], 32768\n\t"
            "s_cbranch_vccnz 3b\n\t"
            "buffer_load_dwordx4 %[v0], %[srd], %[so] offen nt lds\n\t"
            "buffer_load_dwordx4 %[v1], %[srd], %[so] offen offset:1024 nt lds\n\t"
            "buffer_load_dwordx4 %[v2], %[srd], %[so] offen offset:2048 nt lds\n\t"
            "buffer_load_dwordx4 %[v3], %[srd], %[so] offen offset:3072 nt lds\n\t"
            "s_add_u32 m0, %[lp], 36864\n\t"
            "s_add_u32 %[so], %[so], %[step]\n\t"
            "buffer_load_dwordx4 %[v0], %[srd], %[so] offen nt lds\n\t"
            "buffer_load_dwordx4 %[v1], %[srd], %[so] offen offset:1024 nt lds\n\t"
            "buffer_load_dwordx4 %[v2], %[srd], %[so] offen offset:2048 nt lds\n\t"
            "buffer_load_dwordx4 %[v3], %[srd], %[so] offen offset:3072 nt lds\n\t"
            "ds_read_b32 %[pg], %[pa]\n\t"
            "s_memrealtime vcc\n\t"
            "s_add_u32 %[so], %[so], %[step]\n\t"
            "s_add_u32 %[lp], %[lp], 0x2000\n\t"
            "s_and_b32 %[lp], %[lp], 0x1ffff\n\t"
            "s_add_u32 %[u], %[u], 2\n\t"
            "s_mul_i32 %[t2], %[pub], %[tau]\n\t"
            "s_lshr_b32 %[t2], %[t2], 1\n\t"
            "s_add_u32 %[t2], %[t2], %[dead]\n\t"
            "s_sub_i32 %[t], %[u], " STR(W3) "\n\t"
            "s_waitcnt vmcnt(4*" STR(W3) ") lgkmcnt(0)\n\t"
            "s_sub_u32 vcc_lo, vcc_lo, %[t2]\n\t"
            "s_cmp_gt_i32 vcc_lo, " STR(TH3) "\n\t"
            "s_cbranch_scc1 2f\n\t"
            "s_waitcnt vmcnt(4*" STR(W2) ")\n\t"
            "s_sub_i32 %[t], %[u], " STR(W2) "\n\t"
            "s_cmp_gt_i32 vcc_lo, " STR(TH2) "\n\t"
            "s_cbranch_scc1 2f\n\t"
            "s_waitcnt vmcnt(4*" STR(W1) ")\n\t"
            "s_sub_i32 %[t], %[u], " STR(W1) "\n\t"
            "s_cmp_gt_i32 vcc_lo, " STR(TH1) "\n\t"
            "s_cbranch_scc1 2f\n\t"
            "s_waitcnt vmcnt(4*" STR(W0) ")\n\t"
            "s_sub_i32 %[t], %[u], " STR(W0) "\n"
            "2:\n\t"
            "s_max_i32 %[pub], %[pub], %[t]\n\t"
            "v_mov_b32 %[tv], %[pub]\n\t"
            "s_cmp_lt_i32 %[u], %[ue]\n\t"
            "ds_write_b32 %[zv], %[tv] offset:48\n\t"
            "s_cbranch_scc1 1b"
            : [t] "=&s"(t), [tv] "=&v"(tv), [u] "+s"(u), [so] "+s"(so), [lp] "+s"(lp), [t2] "=&s"(t2), [pub] "+s"(pub), [pg] "+v"(pg)
            : [v0] "v"(vo[0]), [v1] "v"(vo[1]), [v2] "v"(vo[2]), [v3] "v"(vo[3]), [srd] "s"(srd), [step] "s"(step), [ue] "s"(uend),
              [pa] "v"(pgaddr), [zv] "v"(zv), [tau] "s"(tau), [dead] "s"(dead)
            : "memory", "vcc", "scc");
        asm volatile("" : "+s"(u), "+s"(so), "+s"(lp), "+s"(pub));
        PIN(pg);
      }
    };
#define PUBLISH(n) do { pub = max(pub, n); lds_post<L_FULL>(zv, pub); } while (0)
    {
      int so = cb * 262144;
      const u16* src = kc;
      int tau = TAU2K;
#pragma unroll 1
      for (int ph = 0; ph < 3; ++ph) {
        run(src, 4 * SK * DH * 2, so, 4096, ph ? 64 * ph : UINIT, tau);
        if (ph == 0) { dead += 1 << 28; wg_barrier(); }
        else { src = vc; so = cb * 262144; tau = TAU2; dead += (TAU2K - TAU2) * 32; }
      }
    }
    if (!isY) {
      VM(32); PUBLISH(U_W - 8); VM(16); PUBLISH(U_W - 4); VM(8); PUBLISH(U_W - 2); VM(0); PUBLISH(U_W);
    } else {
#pragma unroll
      for (int i = 0; i < 4; ++i) vo[i] = (4 * i + g4) * 2048 + r16 * 16 - 1024 * i;
      int so = yi * 65536;
      dead += 1 << 28;
#pragma unroll 1
      for (int ph = 0; ph < 3; ++ph) {
        run(w2, FF * D * 2, so, 256, ph == 0 ? U_W + WPRE : U_W + 8 * ph, 0);
        if (ph == 0) {
          VM(4 * WPRE); PUBLISH(U_W);
          lds_wait<C_PUB, 1>(zv, 8);
          dead -= 1 << 29;
        } else {
          so = yi * 65536 + 32768;
        }
      }
      VM(16); PUBLISH(U_WEND - 4); VM(0); PUBLISH(U_WEND);
    }
    STAMPW(29);
    __builtin_amdgcn_endpgm();
  }
  if (__builtin_expect(w > 8, 1)) {
    const int s = w - 9, st = tid - 576;
    __builtin_amdgcn_s_setprio(PRS);
    const rsrc_t rC = mk(cnd, D * 2), rX = mk(x, D * 2);
    const int f1i = (cb >> 6) * 16 + (cb & 15), f1k = f1i >> 3, f1c = (f1i & 4) * 4 + ((cb >> 4) & 3) * 4 + (f1i & 3);
    const int g2o = WS_G2 + ((cb & 63) * 4 + (cb >> 6)) * 256;
    v4u f1w[4], f2w[8];
    {
      const rsrc_t rMod = mk(wmod, D * 6 * D * 2);
      const int vo = rg * 12288 + (lane & 7) * 16, so = (f1k * 128 + 32 * s) * 12288 + f1c * 128;
#pragma unroll
      for (int i = 0; i < 4; ++i) f1w[i] = ld128<AUX_NT>(rMod, vo, so + i * 8 * 12288);
    }
    const unsigned c1 = ld16(rC, (lane & 31) * 2, (f1k * 128 + 32 * s) * 2);
    const unsigned epoch = ld32<AUX_SC1>(rW, 0, WS_EPOCH);
    st128(v4u{SENT, SENT, SENT, SENT}, rW, st * 16, WS_SENT + cb * 8192);
    st128(v4u{SENT, SENT, SENT, SENT}, rW, st * 16, WS_SENT + cb * 8192 + 4096);
    st128(v4u{SENT, SENT, SENT, SENT}, rW, st < 16 ? g2o + st * 16 : st < 32 ? WS_U + cb * 256 + (st - 16) * 16 : (st < 48 && isX) ? g3o + (st - 32) * 16 : OOB, 0);
    SB();
    const unsigned c3 = ld32<0>(rC, st * 4, (ri & 1) * 1024);
    const v4u xa = ld128<0>(rX, lane * 32, 0), xb = ld128<0>(rX, lane * 32, 16);
    const unsigned xh = ld32<0>(rX, (lane & 31) * 4, ((cb >> 6) * 256 + 64 * s) * 2);
#define F2LOAD() do { \
      const rsrc_t rQ = mk(wq, D * 4096 * 2); \
      const int vo = rg * 8192 + (lane & 7) * 16, so = ((cb >> 6) * 256 + 64 * s) * 8192 + (cb & 63) * 128; \
      _Pragma("unroll") for (int i = 0; i < 8; ++i) f2w[i] = ld128<AUX_NT>(rQ, vo, so + i * 8 * 8192); \
      SB(); } while (0)
    if (F2POS == 0) F2LOAD();
    wg_barrier();
    LAS float* const ex = (LAS float*)(L + L_F1 + 1024 + s * 256);
    const unsigned tag = epoch + 1u;
    {
      if (F2POS == 1) { VM(8); if (s == 0) STAMPW(40); F2LOAD(); }
      ex[(lane & 7) * 4 + ((lane >> 3) & 3)] = silu_bf(bflo(c1));
      CFENCE();
      const f32x4 sv = *(const LAS f32x4*)(ex + rg * 4);
      float acc[8] = {0, 0, 0, 0, 0, 0, 0, 0};
#pragma unroll
      for (int i = 0; i < 4; ++i) fma8(acc, f1w[i], sv[i]);
      if (s == 0) STAMPW(41);
      VM(F2POS == 2 ? 4 : 12);
      if (s == 0) STAMPW(42);
      if (publish64<C_F1, true>(L, zv, L_F1, acc, s, lane, tag, rW, WS_G1 + (f1c * 8 + f1k) * 512)) STAMPW(1);
      if (F2POS == 2) F2LOAD();
    }
    float ln_m, ln_r;
    {
      float v[16];
#pragma unroll
      for (int j = 0; j < 4; ++j) { v[2 * j] = bflo(xa[j]); v[2 * j + 1] = bfhi(xa[j]); v[8 + 2 * j] = bflo(xb[j]); v[9 + 2 * j] = bfhi(xb[j]); }
      float sm = 0.f;
#pragma unroll
      for (int j = 0; j < 16; ++j) sm += v[j];
      const float mean = wave_sum(sm) * (1.f / D);
      float q = 0.f;
#pragma unroll
      for (int j = 0; j < 16; ++j) { const float d = v[j] - mean; q = fmaf(d, d, q); }
      ln_m = rbf(mean); ln_r = ln_rstd(wave_sum(q) * (1.f / D));
    }
    {
      const int g_vo = (lane >> 5) * 65536 + (lane & 31) * 16, g_so = WS_G1 + ((cb >> 6) * 4 + s) * 4096;
      float s0 = 0.f, s1 = 0.f;
      bool bad = true;
      VM(0);
#pragma unroll 1
      for (;;) {
        const int o = bad ? g_vo : OOB;
        v4u g[8];
#pragma unroll
        for (int i = 0; i < 8; ++i) g[i] = ld128<AUX_SC1>(rW, o + i * 512, g_so);
        bool nb = false;
#pragma unroll
        for (int i = 0; i < 8; ++i) nb |= (g[i].y != tag) | (g[i].w != tag);
        const float a0 = ((u2f(g[0].x) + u2f(g[1].x)) + (u2f(g[2].x) + u2f(g[3].x))) + ((u2f(g[4].x) + u2f(g[5].x)) + (u2f(g[6].x) + u2f(g[7].x)));
        const float a1 = ((u2f(g[0].z) + u2f(g[1].z)) + (u2f(g[2].z) + u2f(g[3].z))) + ((u2f(g[4].z) + u2f(g[5].z)) + (u2f(g[6].z) + u2f(g[7].z)));
        s0 = bad ? a0 : s0; s1 = bad ? a1 : s1;
        bad = bad && nb;
        if (!wave_any(bad) || ++spins >= SPIN_CAP) break;
        __builtin_amdgcn_s_sleep(16); CFENCE(); RETRY(22);
      }
      if (s == 0) STAMPW(2);
      STAMPW(43);
      s0 = rbf(s0); s1 = rbf(s1);
      const auto e0s = __builtin_amdgcn_permlane32_swap(f2u(s0), f2u(s0), false, false);
      const auto e1s = __builtin_amdgcn_permlane32_swap(f2u(s1), f2u(s1), false, false);
      const float h0 = rbf(rbf(rbf(rbf(bflo(xh) - ln_m) * ln_r) * rbf(1.f + u2f(e0s[1]))) + u2f(e0s[0]));
      const float h1 = rbf(rbf(rbf(rbf(bfhi(xh) - ln_m) * ln_r) * rbf(1.f + u2f(e1s[1]))) + u2f(e1s[0]));
      LAS float* hp = ex + ((2 * lane) & 7) * 8 + ((lane & 31) >> 2);
      hp[0] = h0; hp[8] = h1;
      CFENCE();
      const f32x4 ha = *(const LAS f32x4*)(ex + rg * 8), hb = *(const LAS f32x4*)(ex + rg * 8 + 4);
      float acc[8] = {0, 0, 0, 0, 0, 0, 0, 0};
#pragma unroll
      for (int i = 0; i < 8; ++i) fma8(acc, f2w[i], i < 4 ? ha[i & 3] : hb[i & 3]);
      if (publish64<C_F2, false>(L, zv, L_F2, acc, s, lane, tag, rW, g2o)) STAMPW(3);
    }
    {
      lds_wait<L_FULL>(zv, GATE_Q);
      lds_wait<C_F2, 1>(zv, 4);
      const int g_vo = (st >> 4) * 1024 + (st & 15) * 16;
      v4u g[4];
      GATHER4(g, g_vo, g_vo + 512, 256, WS_G2 + grp * 16384, 24);
      const f32x4 q4 = (asf(g[0]) + asf(g[1])) + (asf(g[2]) + asf(g[3]));
      *(LAS v2u*)(L + L_Q + st * 8) = v2u{pk2(q4[0], q4[1]), pk2(q4[2], q4[3])};
      if (arrive_rtn<C_Q>(zv) == 3u) {
        if (cb == 0 && lane == 0) __builtin_amdgcn_raw_buffer_store_b32(tag, rW, 0, WS_EPOCH, AUX_SC1);
        STAMPW(4);
      }
      lds_wait<C_Q, 1>(zv, 4);
    }
    if (isX) {
      LAS float* const sx = (LAS float*)(L + L_H + s * 512);
#pragma unroll 1
      for (int j = 0; j < 2; ++j) {
        const int r = 2 * lane + j;
        sx[(r >> 5) * 32 + (r & 7) * 4 + ((r >> 3) & 3)] = silu_bf(j ? bfhi(c3) : bflo(c3));
      }
      CFENCE();
      const rsrc_t rMod = mk(wmod, D * 6 * D * 2);
      const int vo = rg * 12288 + (lane & 7) * 16;
      int so = ((ri & 1) * 512 + 128 * s) * 12288 + 4096 + (ri >> 1) * 128;
      float acc[8] = {0, 0, 0, 0, 0, 0, 0, 0};
#pragma unroll 1
      for (int j = 0; j < 2; ++j, so += 64 * 12288) {
        v4u mw[8];
#pragma unroll
        for (int i = 0; i < 8; ++i) mw[i] = ld128<AUX_NT>(rMod, vo, so + i * 8 * 12288);
        SB();
        const f32x4 sv = *(const LAS f32x4*)(sx + j * 64 + rg * 4), sw = *(const LAS f32x4*)(sx + j * 64 + 32 + rg * 4);
#pragma unroll
        for (int i = 0; i < 8; ++i) fma8(acc, mw[i], i < 4 ? sv[i & 3] : sw[i & 3]);
      }
      if (publish64<C_F3, false>(L, zv, L_F3, acc, s, lane, 0u, rW, g3o)) STAMPW(40);
    }
    lds_wait<L_FULL>(zv, GATE_G3);
    if (isY) {
      const int va = 2 * 8192 + (st >> 4) * 512 + (st & 15) * 16, vb = va - 8192;
      v4u g[4];
      GATHER4(g, va, vb, 256, WS_G3, 23);
      const f32x4 cr = asf(g[0]) + asf(g[1]), sr = asf(g[2]) + asf(g[3]);
      const v2u cp = {pk2(1.f + rbf(cr[0]), 1.f + rbf(cr[1])), pk2(1.f + rbf(cr[2]), 1.f + rbf(cr[3]))}, sq = {pk2(sr[0], sr[1]), pk2(sr[2], sr[3])};
      *(LAS v2u*)(L + L_CS + st * 8) = cp;
      *(LAS v2u*)(L + L_CS + 2048 + st * 8) = sq;
      float kc5[5] = {0, 0, 0, 0, 0};
#pragma unroll
      for (int j = 0; j < 4; ++j) {
        const float c = (j & 1) ? bfhi(cp[j >> 1]) : bflo(cp[j >> 1]), sv = (j & 1) ? bfhi(sq[j >> 1]) : bflo(sq[j >> 1]);
        kc5[0] += c; kc5[1] += sv; kc5[2] = fmaf(c, c, kc5[2]); kc5[3] = fmaf(c, sv, kc5[3]); kc5[4] = fmaf(sv, sv, kc5[4]);
      }
#pragma unroll
      for (int i = 0; i < 5; ++i) { const float t = wave_sum(kc5[i]); if (lane == 0) ((LAS float*)(L + L_F1 + 1536))[i * 4 + s] = t; }
      arrive<C_CS>(zv);
    } else if (isRed && s == 1) {
      const int e = 32 * rj + 4 * (lane & 7), va = ((lane >> 3) & 3) * 8192 + (e >> 6) * 512 + (e & 63) * 4;
      const unsigned xv = ld16(rX, (lane & 31) * 2, rj * 64), bv = ld16(mk(b2, D * 2), (lane & 31) * 2, rj * 64);
      v4u g[4];
      GATHER4(g, va, va, 256, WS_G3, 23);
      const f32x4 m = asf(g[0]) + asf(g[1]);
      const float one = ((lane >> 3) & 3) == 2 ? 1.f : 0.f;
      if (lane < 32) *(LAS f32x4*)(L + L_CS + lane * 16) = f32x4{rbf(one + rbf(m[0])), rbf(one + rbf(m[1])), rbf(one + rbf(m[2])), rbf(one + rbf(m[3]))};
      ((LAS float*)(L + L_CS + 512))[lane] = bflo(lane < 32 ? xv : bv);
      arrive<C_CS>(zv);
    }
    if (s == 0) {
      if (isX) {
        lds_wait<L_FULL>(zv, GATE_ST);
        v4u g;
        _Pragma("unroll 1") for (;;) {
          g = ld128<AUX_SC1>(rW, lane * 128, WS_ST + grp * 8192 + ((ri >> 3) & 3) * 16);
          if (!wave_any(max4(g) == SENT) || ++spins >= SPIN_CAP) break;
          __builtin_amdgcn_s_sleep(16); CFENCE(); RETRY(26);
        }
        const float e0 = ex2((u2f(g.x) - wave_max(u2f(g.x))) * LOG2E), e1 = ex2((u2f(g.z) - wave_max(u2f(g.z))) * LOG2E);
        *(LAS f32x2*)(L + L_H + lane * 8) = f32x2{e0 * rcp(wave_sum(e0 * u2f(g.y))), e1 * rcp(wave_sum(e1 * u2f(g.w)))};
        arrive<C_ML>(zv);
        STAMPW(17);
      }
      if (isY) {
        lds_wait<C_CS>(zv, 4);
        float kcs[5];
#pragma unroll
        for (int i = 0; i < 5; ++i) { const f32x4 t = *(const LAS f32x4*)(L + L_F1 + 1536 + i * 16); kcs[i] = (t[0] + t[1]) + (t[2] + t[3]); PIN(kcs[i]); }
        lds_wait<L_FULL, 4>(zv, U_WEND);
        v4u gx;
        _Pragma("unroll 1") do {
          CFENCE();
          gx = ld128<AUX_SC1>(rW, (lane & 31) * 128 + (lane >> 5) * 16, WS_U + (cb >> 5) * U_REP + 4096);
        } while (wave_any(max4(gx) == SENT) && ++spins < SPIN_CAP);
        const float h0 = half_sum(u2f(gx.x)), h1 = half_sum(u2f(gx.y)), h2 = half_sum(u2f(gx.z));
        const auto a0 = __builtin_amdgcn_permlane32_swap(f2u(h0), f2u(h0), false, false);
        const auto a1 = __builtin_amdgcn_permlane32_swap(f2u(h1), f2u(h1), false, false);
        const auto a2 = __builtin_amdgcn_permlane32_swap(f2u(h2), f2u(h2), false, false);
        const float sx = u2f(a0[0]), sxx = u2f(a1[0]), su = u2f(a2[0]), suu = u2f(a0[1]), suc = u2f(a1[1]), sus = u2f(a2[1]);
        const float m1 = sx * (1.f / D);
        const float r1 = ln_rstd(fmaxf(fmaf(sxx, 1.f / D, -m1 * m1), 0.f)), m1b = rbf(m1);
        const float A1 = fmaf(-m1b, kcs[0], su), A2 = fmaf(m1b, fmaf(m1b, kcs[2], -2.f * suc), suu), A3 = fmaf(-m1b, kcs[3], sus);
        const float m2 = fmaf(r1, A1, kcs[1]) * (1.f / D);
        const float e2 = fmaf(r1, fmaf(r1, A2, 2.f * A3), kcs[4]) * (1.f / D);
        const float r2 = ln_rstd(fmaxf(fmaf(-m2, m2, e2), 0.f)), m2b = rbf(m2);
        const float a = r2 * r1;
        if (lane == 0) *(LAS f32x4*)(L + L_ST) = f32x4{a, -a * m1b, r2, -r2 * m2b};
        STAMPW(19);
        wg_barrier();
      }
    }
    __builtin_amdgcn_endpgm();
  }
  {
    __builtin_amdgcn_s_setprio(PRW);
    wg_barrier();
    LAS unsigned char* const prog = L + L_PROG + w * 4;
#define RELEASE(next) do { LGKM0(); *(volatile LAS int*)prog = (next); } while (0)
    const int efrag = 8 * g4 + (lane & 7);
    unsigned k_b;
    v4u wr[8], wf[8];
    const rsrc_t rR = mk(isX ? wo : w1, isX ? 4096 * D * 2 : isY ? D * FF * 2 : 0);
    const int r_vo = isX ? ((g4 >> 1) * 128 + (g4 & 1) * 8) * 2048 + r16 * 16 : g4 * 8 * (FF * 2) + r16 * 4;
    const int r_so = isX ? ((grp * 8 + 2 * ((ri >> 3) & 3)) * 128 + 16 * (ri & 7)) * 2048 + w * 256 : isY ? 128 * w * (FF * 2) + yi * 64 : 0;
#define W1LOAD(i) wr[(i) >> 2][(i) & 3] = ld32<W1AUX>(rR, r_vo, r_so + (((i) >> 3) * 32 + ((i) & 7)) * (FF * 2))
#define RLOAD(m) do { if (isX) wr[m] = ld128<AUX_NT>(rR, r_vo, r_so + (m) * 2048); else { W1LOAD(4 * (m)); W1LOAD(4 * (m) + 1); W1LOAD(4 * (m) + 2); W1LOAD(4 * (m) + 3); } SB(); } while (0)
    k_b = ld16(mk(b1, isY ? FF * 2 : 0), efrag * 2 + yi * 64, 0);
    {
      const int ka = r16 * 256 + ((g4 ^ kvswz(r16)) * 16);
      v4u kp[4][4];
      RLOAD(0);
#pragma unroll
      for (int m = 0; m < 4; ++m) {
        const int u = U_K + 8 * m + w;
        lds_wait<L_FULL>(zv, u + 1);
#pragma unroll
        for (int ks = 0; ks < 4; ++ks) kp[m][ks] = *(const LAS v4u*)(L + L_RING + (u & 31) * 4096 + (ka ^ (ks * 64)));
        RELEASE(u + 8);
        if (m == 1) asm volatile("" : "+v"(k_b) : "v"(pfc));
        RLOAD(m + 1);
      }
      STAMP(18);
      lds_wait<C_Q, 2>(zv, 4);
      v4u qf[4];
#pragma unroll
      for (int ks = 0; ks < 4; ++ks) qf[ks] = *(const LAS v4u*)(L + L_Q + (r16 & 7) * 256 + ks * 64 + g4 * 16);
      const int sw = L_S + (w >> 1) * 512 + (g4 * 8 + (r16 & 7)) * 16 + (w & 1) * 8;
#define QKSTEP(kf, m) do { \
        f32x4 d = {0, 0, 0, 0}; \
        _Pragma("unroll") for (int ks = 0; ks < 4; ++ks) d = mfma(kf[ks], qf[ks], d); \
        const unsigned a = pk2(d[0], d[1]), b = pk2(d[2], d[3]); \
        constexpr float SCALE = 0.08838834764831845f; \
        *(LAS v2u*)(L + sw + (m) * 2048) = v2u{pk2(bflo(a) * SCALE, bfhi(a) * SCALE), pk2(bflo(b) * SCALE, bfhi(b) * SCALE)}; } while (0)
#pragma unroll
      for (int m = 0; m < 4; ++m) QKSTEP(kp[m], m);
#pragma unroll
      for (int m = 4; m < 8; ++m) {
        const int u = U_K + 8 * m + w;
        lds_wait<L_FULL, 4>(zv, u + 1);
        if (m == 7 && w == 7) STAMPW(58);
        v4u kf[4];
#pragma unroll
        for (int ks = 0; ks < 4; ++ks) kf[ks] = *(const LAS v4u*)(L + L_RING + (u & 31) * 4096 + (ka ^ (ks * 64)));
        RELEASE(m == 7 ? U_V : u + 8);
        if (m < 7) RLOAD(m + 1);
        if (m == 5 && w == 0) { const unsigned tv = ld32<AUX_NT>(mk(vc, 4 * SK * DH * 2), (lane & 3) * 65536, cb * 262144); asm volatile("" ::"v"(tv)); }
        QKSTEP(kf, m);
      }
    }
    {
      LAS unsigned char* const sp_ = L + L_S + ((lane >> 5) * 4 + (w >> 1)) * 512 + (lane & 31) * 16 + (w & 1) * 8;
      CFENCE();
      float sc[16];
      float mx = -INFINITY;
#pragma unroll
      for (int i = 0; i < 4; ++i) {
        const v2u s = *(const LAS v2u*)(sp_ + i * 4096);
        sc[4 * i] = bflo(s.x); sc[4 * i + 1] = bfhi(s.x); sc[4 * i + 2] = bflo(s.y); sc[4 * i + 3] = bfhi(s.y);
      }
#pragma unroll
      for (int i = 0; i < 16; ++i) mx = fmaxf(mx, sc[i]);
      mx = max_hi3(mx);
      if (lane < 8) ((LAS float*)(L + L_ST))[w * 8 + lane] = mx;
      if (w == 7) STAMPW(53);
      arrive<C_ST>(zv);
      lds_wait<C_ST, SLPC>(zv, 8);
      if (w == 7) STAMPW(54);
      if (w == 0) STAMPW(55);
      const float M = max_hi3(((const LAS float*)(L + L_ST))[lane]);
      float l = 0.f;
#pragma unroll
      for (int i = 0; i < 4; ++i) {
        float p[4];
#pragma unroll
        for (int j = 0; j < 4; ++j) { p[j] = ex2((sc[4 * i + j] - M) * LOG2E); l += p[j]; }
        *(LAS v2u*)(sp_ + i * 4096) = v2u{pk2(p[0], p[1]), pk2(p[2], p[3])};
      }
      l = sum_hi3(l);
      if (w == 7) STAMPW(56);
      if (w == 0) STAMPW(57);
      if (lane < 8) ((LAS float*)(L + L_ST + 256))[w * 8 + lane] = l;
      if (arrive_rtn<C_P>(zv) == 7u) {
        const float Ls = sum_hi3(((const LAS float*)(L + L_ST + 256))[lane]);
        const v4u o = {nosent(M), nosent(Ls), nosent(dppf<SHL1>(M)), nosent(dppf<SHL1>(Ls))};
        if (lane < 8 && !(lane & 1)) st128(o, rW, lane * 8, WS_ST + cb * 128);
        STAMPW(5);
      }
    }
    if (isX) {
#pragma unroll
      for (int jd = 0; jd < 8; ++jd) wf[jd] = tr8(wr, jd);
    } else {
#pragma unroll
      for (int ks = 0; ks < 4; ++ks)
#pragma unroll
        for (int u = 0; u < 4; ++u) {
          const unsigned lo = wr[2 * ks + (u >> 1)][(2 * u) & 3], hi = wr[2 * ks + (u >> 1)][(2 * u + 1) & 3];
          wf[2 * ks][u] = __builtin_amdgcn_perm(hi, lo, 0x05040100u); wf[2 * ks + 1][u] = __builtin_amdgcn_perm(hi, lo, 0x07060302u);
        }
    }
    {
      const int vrow = 4 * g4 + (r16 >> 2);
      const int va = vrow * 256 + (((2 * w + ((lane >> 1) & 1)) ^ kvswz(vrow)) * 16) + (lane & 1) * 8;
      const int pa = L_S + (g4 * 8 + (r16 & 7)) * 16;
      unsigned full = 0;
#define VREAD(dst, t, S) do { \
        const int u_ = U_V + 2 * (t); \
        if (full < (unsigned)(u_ + 2)) full = lds_wait<L_FULL, S>(zv, u_ + 2); \
        LAS unsigned char* p_ = L + L_RING + (u_ & 31) * 4096 + va; \
        const v2u v0_ = __builtin_bit_cast(v2u, __builtin_amdgcn_ds_read_tr16_b64_v4i16((LAS v4s*)p_)); \
        const v2u v1_ = __builtin_bit_cast(v2u, __builtin_amdgcn_ds_read_tr16_b64_v4i16((LAS v4s*)(p_ + 4096))); \
        dst = v4u{v0_.x, v0_.y, v1_.x, v1_.y}; } while (0)
      lds_wait<C_P, 1>(zv, 8);
      STAMP(6);
      f32x4 acc = {0, 0, 0, 0};
      int pa_vo = r16 < 8 ? (r16 >> 1) * 8 * STR_A + (r16 & 1) * 64 + g4 * 16 : OOB, pa_so = WS_PA + (grp * 32 + w) * STR_A + sp * 128;
      PIN(pa_vo); asm volatile("" : "+s"(pa_so));
#pragma unroll 1
      for (int t = 0; t < 28; t += 4) {
        v4u pf[4], vf[4];
#pragma unroll
        for (int i = 0; i < 4; ++i) pf[i] = *(const LAS v4u*)(L + pa + (t + i) * 512);
        VREAD(vf[3], t + 3, 4);
#pragma unroll
        for (int i = 0; i < 3; ++i) VREAD(vf[i], t + i, 2);
#pragma unroll
        for (int i = 0; i < 4; ++i) acc = mfma(vf[i], pf[i], acc);
        RELEASE(U_V + 2 * t + 8);
      }
#pragma unroll 1
      for (int t = 28; t < 32; ++t) {
        const v4u pfr = *(const LAS v4u*)(L + pa + t * 512);
        v4u vf;
        VREAD(vf, t, 1);
        acc = mfma(vf, pfr, acc);
      }
      st128(v4u{nosent(acc[0]), nosent(acc[1]), nosent(acc[2]), nosent(acc[3])}, rW, pa_vo, pa_so);
      SB();
      if (isY) arrive<C_PUB>(zv);
    }
    STAMPW(7);
    if (isX || isY) {
      int t_wr = L_F1 + (4 * (lane & 7) + (g4 & 1) * 2 + (g4 >> 1)) * 48 + w * 4;
      int t_rd = L_F1 + efrag * 48;
      int o_vo = (g4 & 1) ? OOB : (r16 >> 2) * STR_B + (r16 & 3) * 32 + (g4 >> 1) * 16;
      int o_so = (isX ? WS_PB + ri * 128 : WS_PD + yi * 128) + 4 * w * STR_B;
      int g_vo = tid * 16;
      PIN(t_wr); PIN(t_rd); PIN(o_vo); PIN(g_vo); asm volatile("" : "+s"(o_so));
      float kC = 0.f, kZ = 0.f, kO = 0.f, kB = 0.f;
      if (isX) {
        lds_wait<C_ML, 1>(zv, 1);
        float k_w = ((const LAS float*)(L + L_H))[(tid >> 3) * 2 + ((tid >> 2) & 1)];
        PIN(k_w);
        v4u g;
        _Pragma("unroll 1") do {
          CFENCE();
          g = ld128<AUX_SC1>(rW, g_vo, WS_PA + ri * STR_A);
        } while (wave_any(max4(g) == SENT) & (++spins < SPIN_CAP));
        *(LAS float*)(L + t_wr) = reduce4(asf(g) * k_w);
      }
      int yrole = cb & 1;
      asm volatile("" : "+s"(yrole));
      if (yrole) {
        {
          lds_wait<C_CS>(zv, 4);
          f32x4 h0 = {0, 0, 0, 0}, h1 = {0, 0, 0, 0};
#pragma unroll
          for (int ks = 0; ks < 4; ++ks) {
            v4u a = *(const LAS v4u*)(L + L_CS + (r16 & 1) * 2048 + w * 256 + ks * 64 + g4 * 16);
            if (r16 & 2) a = v4u{0x3F803F80u, 0x3F803F80u, 0x3F803F80u, 0x3F803F80u};
            h0 = mfma(a, wf[2 * ks], h0); h1 = mfma(a, wf[2 * ks + 1], h1);
          }
          if (lane < 16) {
            LAS float* p = (LAS float*)(L + L_F2) + lane * 56 + w;
            p[0] = h0[0]; p[8] = h0[1]; p[16] = h0[2]; p[28] = h1[0]; p[36] = h1[1]; p[44] = h1[2];
          }
          arrive<C_PRE>(zv);
        }
        lds_wait<C_PRE, 1>(zv, 8);
        kC = sum8(L + L_F2 + efrag * 112); kZ = sum8(L + L_F2 + efrag * 112 + 32); kO = sum8(L + L_F2 + efrag * 112 + 64);
        kB = bflo(k_b);
        PIN(kC); PIN(kZ); PIN(kO); PIN(kB);
        v4u f1[8];
#pragma unroll
        for (int i = 0; i < 8; ++i)
#pragma unroll
          for (int u = 0; u < 4; ++u) asm volatile("v_mov_b32 %0, %1" : "=&v"(f1[i][u]) : "v"(wf[i][u]));
        lds_wait<L_FULL, 2>(zv, U_W + 8 + w + 1);
        {
          v4u w2r[8];
#pragma unroll
          for (int i = 0; i < 8; ++i) w2r[i] = *(const LAS v4u*)(L + L_RING + ((U_W + 8 * (g4 >> 1) + w) & 31) * 4096 + (g4 & 1) * 2048 + r16 * 16 + i * 256);
#pragma unroll
          for (int jd = 0; jd < 8; ++jd) { wf[jd] = tr8(w2r, jd); PIN(wf[jd]); }
        }
        int h_wr = L_F1 + r16 * 96 + w * 4, u_vo = (r16 & 1) * 64 + g4 * 16;
        PIN(h_wr); PIN(u_vo);
        lds_wait<L_FULL, 2>(zv, U_WEND);
        STAMP(16);
        v4u ua[4];
        _Pragma("unroll 1") do {
          CFENCE();
#pragma unroll
          for (int ks = 0; ks < 4; ++ks) ua[ks] = ld128<AUX_SC1>(rW, u_vo + ks * 128, WS_U + (cb >> 5) * U_REP + w * 512);
        } while (wave_any(max(max(max4(ua[0]), max4(ua[1])), max(max4(ua[2]), max4(ua[3]))) == SENT) & (++spins < SPIN_CAP));
        STAMP(12);
        f32x4 h0 = {0, 0, 0, 0}, h1 = {0, 0, 0, 0};
#pragma unroll
        for (int ks = 0; ks < 4; ++ks) { h0 = mfma(ua[ks], f1[2 * ks], h0); h1 = mfma(ua[ks], f1[2 * ks + 1], h1); }
        *(LAS float*)(L + h_wr) = h0[0] + h0[1]; *(LAS float*)(L + h_wr + 48) = h1[0] + h1[1];
      }
      wg_barrier();
      STAMP(isX ? 8 : 21);
      {
        const f32x4 k = *(const LAS f32x4*)(L + L_ST);
        float v = sum8(L + t_rd);
        if (yrole) {
          const float t = rbf(rbf(fmaf(k[0], v, fmaf(k[1], kC, fmaf(k[2], kZ, k[3] * kO)))) + kB);
          constexpr float C1 = -2.f * 0.7978845608028654f * LOG2E;
          v = t * rcp(1.f + ex2(t * fmaf(t * t, C1 * 0.044715f, C1)));
        }
        const v4u af = frag_from_lanes(v);
        f32x4 d[8];
#pragma unroll
        for (int jd = 0; jd < 8; ++jd) d[(jd >> 1) + (jd & 1) * 4] = mfma(af, wf[(jd >> 1) + (jd & 1) * 4], f32x4{0, 0, 0, 0});
        const bool hi = lane >= 32;
        st128(v4u{nosent(hi ? d[4][0] : d[0][0]), nosent(hi ? d[5][0] : d[1][0]), nosent(hi ? d[6][0] : d[2][0]), nosent(hi ? d[7][0] : d[3][0])}, rW, o_vo, o_so);
      }
      STAMP(isX ? 9 : 13);
      if (isRed) {
        lds_wait<C_CS, 1>(zv, 1);
        const LAS float* kp = (const LAS float*)(L + L_CS) + (lane & 31);
        float k_ga = kp[0], k_s = kp[32], k_c = kp[64], k_gb = kp[96], k_x = kp[128], k_b2 = kp[160];
        int r_rd = L_F2 + (lane & 31) * 48;
        int c_vo = w == 0 ? (lane & 7) * U_REP + (lane >> 3) * 16 : (lane & 24) ? OOB : (lane & 7) * U_REP + 4096 + (lane >> 5) * 16;
        int f_vo = (lane & 39) ? OOB : lane * 2, g2_vo = g_vo + 8192;
        PIN(k_ga); PIN(k_s); PIN(k_c); PIN(k_gb); PIN(k_x); PIN(k_b2); PIN(r_rd); PIN(c_vo); PIN(f_vo); PIN(g2_vo);
        const bool hi = lane >= 32;
        v4u ga, gb;
        _Pragma("unroll 1") do {
          CFENCE();
          ga = ld128<AUX_SC1>(rW, g_vo, WS_PB + rj * STR_B); gb = ld128<AUX_SC1>(rW, g2_vo, WS_PB + rj * STR_B);
        } while (wave_any(max(max4(ga), max4(gb)) == SENT) & (++spins < SPIN_CAP));
        *(LAS float*)(L + t_wr + 2048) = reduce4(asf(ga) + asf(gb));
        wg_barrier();
        STAMP(10);
        const float x2 = rbf(k_x + rbf(k_ga * rbf(sum8(L + r_rd))));
        if (w < 2) {
          const float uu = x2 * k_c;
          v4u o;
          if (w == 0) {
            const v4u f = frag_from_lanes(hi ? uu - rbf(uu) : uu);
#pragma unroll
            for (int i = 0; i < 4; ++i) {
              const unsigned q = dppu<0x00>(nosentu(f[i]));
              o[i] = (unsigned)__builtin_amdgcn_update_dpp((int)q, (int)q, 0x114, 0xf, 0xa, false);
            }
          } else {
            o = v4u{nosent(half_sum(hi ? uu * uu : x2)), nosent(half_sum(hi ? uu * k_c : x2 * x2)), nosent(half_sum(hi ? uu * k_s : uu)), 0u};
          }
          st128(o, rW, c_vo, WS_U + rj * 128);
        }
        STAMP(11);
        if (w >= 6) g2_vo = OOB;
        _Pragma("unroll 1") do {
          CFENCE();
          ga = ld128<AUX_SC1>(rW, g_vo, WS_PD + rj * STR_B); gb = ld128<AUX_SC1>(rW, g2_vo, WS_PD + rj * STR_B);
        } while (wave_any(max(max4(ga), max4(gb)) == SENT) & (++spins < SPIN_CAP));
        *(LAS float*)(L + t_wr + 4096) = reduce4(asf(ga) + asf(gb));
        wg_barrier();
        STAMP(14);
        if (w == 0) st128(frag_from_lanes(x2 + rbf(k_gb * rbf(rbf(sum8(L + r_rd + 2048)) + k_b2))), mk(out, D * 2), f_vo, rj * 64);
        STAMP(15);
      }
    }
    FINISH();
  }
}

}

extern "C" void kernel_launch(void* const* d_in, const int* in_sizes, int n_in, void* d_out, int out_size, void* d_ws, size_t ws_size,
                              hipStream_t stream) {
  (void)in_sizes; (void)n_in; (void)out_size; (void)ws_size;
  fused_kernel<<<dim3(256), dim3(832), 0, stream>>>(
      (const u16*)d_in[0], (const u16*)d_in[1], (const u16*)d_in[2], (const u16*)d_in[3], (const u16*)d_in[4], (const u16*)d_in[6],
      (const u16*)d_in[7], (const u16*)d_in[8], (const u16*)d_in[9], (const u16*)d_in[10], (const u16*)d_in[11], (u16*)d_out,
      (unsigned char*)d_ws);
}
```

```cpp
#include <hip/hip_runtime.h>
#include <hip/hip_bf16.h>
#include <stdint.h>

typedef unsigned short u16;
typedef unsigned v4u __attribute__((ext_vector_type(4)));
typedef unsigned v2u __attribute__((ext_vector_type(2)));
typedef int v4i __attribute__((ext_vector_type(4)));
typedef short v4s __attribute__((ext_vector_type(4)));
typedef float f32x4 __attribute__((ext_vector_type(4)));
typedef float f32x2 __attribute__((ext_vector_type(2)));
typedef short bf16x8 __attribute__((ext_vector_type(8)));
typedef __bf16 bf16x2_t __attribute__((ext_vector_type(2)));
typedef __amdgpu_buffer_rsrc_t rsrc_t;
#define LAS __attribute__((address_space(3)))

namespace {

constexpr int D = 1024, SK = 65536, DH = 128, FF = 3584;
constexpr int AUX_NT = 2, AUX_SC1 = 16;
constexpr unsigned SENT = 0xFFFFFFFFu;
constexpr int CODE_BYTES = 24 * 1024;
constexpr unsigned SPIN_CAP = 1u << 17;
constexpr float LOG2E = 1.4426950408889634f;

#define TH1 -80
#define TH2 -30
#define TH3 30
constexpr int TOFF = 430;
constexpr int TAU2K = 34, TAU2 = 27;
constexpr int CALAT = 36, PBIAS = 40;
constexpr int UINIT = 4;
#define WPRE 2
constexpr int SHIFT = 8;
constexpr int XLATE = 4, XAT = 2;
constexpr int XA = 135, XC = 142, XD = 108;

constexpr int WS_EPOCH = 0;
constexpr int WS_TS = 1024;
constexpr int WS_G1 = 4096;
constexpr int WS_G2 = WS_G1 + 131072;
constexpr int WS_G3 = WS_G2 + 65536;
constexpr int WS_G4 = WS_G3 + 32768;
constexpr int WS_SENT = 512 * 1024;
constexpr int STR_A = 8192 + 128, STR_B = 16384 + 14336 + 128;
constexpr int WS_PA = WS_SENT;
constexpr int WS_PB = WS_PA + 128 * STR_A;
constexpr int WS_PD = WS_PB + 16384;
constexpr int WS_ST = WS_PB + 32 * STR_B;
constexpr int WS_SENT_END = WS_ST + 32768;
static_assert(WS_SENT_END <= WS_SENT + 2 * 1024 * 1024, "sentinel region");
constexpr int WS_U = WS_G3 + 65536, U_REP = 8192 + 512;
static_assert(WS_U + 8 * U_REP <= WS_SENT, "replicas");
constexpr int WS_BYTES = WS_SENT + 2 * 1024 * 1024;


constexpr int L_CNT = 0;
constexpr int C_F1 = 0, C_F2 = 4, C_F3 = 8, C_Q = 12, C_ST = 16, C_CS = 20, C_P = 24, C_ML = 28, C_F4 = 32, C_PUB = 36, C_PRE = 40, C_TR = 44, L_FULL = 48, L_BASE = 52, L_PROG = 64;
constexpr int L_ST = 128;
constexpr int L_Q = L_ST + 512;
constexpr int L_F1 = L_Q + 2048;
constexpr int L_F2 = L_F1 + 2048;
constexpr int L_F3 = L_F2 + 2048;
constexpr int L_H = L_F3 + 2048;
constexpr int L_S = 12288;
constexpr int L_CS = L_S + 16384;
constexpr int L_RING = 32768;
constexpr int L_BYTES = L_RING + 131072;
static_assert(L_H + 2048 <= L_S && L_RING == 32768 && L_FULL == 48 && L_BASE == 52, "LDS map (the loader loop has these literally)");

__device__ __forceinline__ float u2f(unsigned u) { return __builtin_bit_cast(float, u); }
__device__ __forceinline__ unsigned f2u(float f) { return __builtin_bit_cast(unsigned, f); }
__device__ __forceinline__ float bflo(unsigned p) { return u2f(p << 16); }
__device__ __forceinline__ float bfhi(unsigned p) { return u2f(p & 0xffff0000u); }
__device__ __forceinline__ unsigned pk2(float lo, float hi) {
  f32x2 v = {lo, hi};
  return __builtin_bit_cast(unsigned, __builtin_convertvector(v, bf16x2_t));
}
__device__ __forceinline__ float rbf(float f) { return u2f(pk2(0.f, f)); }
template <int CTRL> __device__ __forceinline__ unsigned dppu(unsigned x) { return (unsigned)__builtin_amdgcn_mov_dpp((int)x, CTRL, 0xf, 0xf, true); }
template <int CTRL> __device__ __forceinline__ float dppf(float x) { return u2f(dppu<CTRL>(f2u(x))); }
constexpr int XOR1 = 0xB1, XOR2 = 0x4E, XOR7 = 0x141, ROR8 = 0x128, SHL1 = 0x101, SHL2 = 0x102, SHL4 = 0x104, SHL6 = 0x106;
__device__ __forceinline__ float sum_sw32(float a, float b) { auto r = __builtin_amdgcn_permlane32_swap(f2u(a), f2u(b), false, false); return u2f(r[0]) + u2f(r[1]); }
__device__ __forceinline__ float sum_sw16(float a, float b) { auto r = __builtin_amdgcn_permlane16_swap(f2u(a), f2u(b), false, false); return u2f(r[0]) + u2f(r[1]); }
__device__ __forceinline__ float max_sw32(float a) { auto r = __builtin_amdgcn_permlane32_swap(f2u(a), f2u(a), false, false); return fmaxf(u2f(r[0]), u2f(r[1])); }
__device__ __forceinline__ float max_sw16(float a) { auto r = __builtin_amdgcn_permlane16_swap(f2u(a), f2u(a), false, false); return fmaxf(u2f(r[0]), u2f(r[1])); }
__device__ __forceinline__ float half_sum(float v) {
  v += dppf<XOR1>(v); v += dppf<XOR2>(v); v += dppf<XOR7>(v); v += dppf<ROR8>(v);
  return sum_sw16(v, v);
}
__device__ __forceinline__ float wave_sum(float v) { v = half_sum(v); return sum_sw32(v, v); }
__device__ __forceinline__ float max_hi3(float v) { v = fmaxf(v, dppf<ROR8>(v)); return max_sw32(max_sw16(v)); }
__device__ __forceinline__ float wave_max(float v) { v = fmaxf(v, dppf<XOR1>(v)); v = fmaxf(v, dppf<XOR2>(v)); v = fmaxf(v, dppf<XOR7>(v)); return max_hi3(v); }
__device__ __forceinline__ float sum_hi3(float v) { v += dppf<ROR8>(v); v = sum_sw16(v, v); return sum_sw32(v, v); }
__device__ __forceinline__ f32x4 mfma(v4u a, v4u b, f32x4 c) {
  return __builtin_amdgcn_mfma_f32_16x16x32_bf16(__builtin_bit_cast(bf16x8, a), __builtin_bit_cast(bf16x8, b), c, 0, 0, 0);
}
__device__ __forceinline__ rsrc_t mk(const void* p, int bytes) { return __builtin_amdgcn_make_buffer_rsrc((void*)p, 0, bytes, 0x00020000); }
template <int AUX> __device__ __forceinline__ v4u ld128(rsrc_t r, int vo, int so) { return __builtin_amdgcn_raw_buffer_load_b128(r, vo, so, AUX); }
template <int AUX> __device__ __forceinline__ v2u ld64(rsrc_t r, int vo, int so) { return __builtin_amdgcn_raw_buffer_load_b64(r, vo, so, AUX); }
template <int AUX> __device__ __forceinline__ unsigned ld32(rsrc_t r, int vo, int so) { return __builtin_amdgcn_raw_buffer_load_b32(r, vo, so, AUX); }
__device__ __forceinline__ unsigned ld16(rsrc_t r, int vo, int so) { return __builtin_amdgcn_raw_buffer_load_b16(r, vo, so, 0); }
__device__ __forceinline__ void st128(v4u v, rsrc_t r, int vo, int so) { __builtin_amdgcn_raw_buffer_store_b128(v, r, vo, so, AUX_SC1); }
__device__ __forceinline__ void st64(v2u v, rsrc_t r, int vo, int so) { __builtin_amdgcn_raw_buffer_store_b64(v, r, vo, so, AUX_SC1); }
#define STR_(x) #x
#define STR(x) STR_(x)
#define SB() __builtin_amdgcn_sched_barrier(0)
#define CFENCE() asm volatile("" ::: "memory")
#define LGKM0() asm volatile("s_waitcnt lgkmcnt(0)" ::: "memory")
#define VM(N) asm volatile("s_waitcnt vmcnt(%0)" ::"i"(N) : "memory")
#define PIN(a) asm volatile("" : "+v"(a))
__device__ __forceinline__ void wg_barrier() {
  LGKM0(); SB();
  __builtin_amdgcn_s_barrier();
  CFENCE(); SB();
}
__device__ __forceinline__ unsigned nosentu(unsigned v) { return min(v, 0xFFFFFFFEu); }
__device__ __forceinline__ unsigned nosent(float v) { return nosentu(f2u(v)); }
__device__ __forceinline__ unsigned max4(v4u v) { return max(max(v.x, v.y), max(v.z, v.w)); }
__device__ __forceinline__ bool wave_any(bool p) { return __builtin_amdgcn_ballot_w64(p) != 0ull; }
__device__ __forceinline__ float rcp(float x) { return __builtin_amdgcn_rcpf(x); }
__device__ __forceinline__ float ex2(float x) { return __builtin_amdgcn_exp2f(x); }
__device__ __forceinline__ float silu_bf(float x) { return rbf(x / (1.f + expf(-x))); }
__device__ __forceinline__ float ln_rstd(float var) { return rcp(rbf(__builtin_amdgcn_sqrtf(rbf(rbf(var) + 1e-5f)))); }

__device__ __forceinline__ void fma8(float (&acc)[8], v4u w, float x) {
#pragma unroll
  for (int j = 0; j < 4; ++j) { acc[2 * j] = fmaf(x, bflo(w[j]), acc[2 * j]); acc[2 * j + 1] = fmaf(x, bfhi(w[j]), acc[2 * j + 1]); }
}
__device__ __forceinline__ float reduce8(const float (&a)[8], int lane) {
  float b0 = sum_sw32(a[0], a[4]), b1 = sum_sw32(a[1], a[5]), b2 = sum_sw32(a[2], a[6]), b3 = sum_sw32(a[3], a[7]);
  float c0 = sum_sw16(b0, b2), c1 = sum_sw16(b1, b3);
  const bool b = lane & 8;
  float keep = b ? c1 : c0, send = b ? c0 : c1;
  return keep + dppf<ROR8>(send);
}
__device__ __forceinline__ float reduce4(f32x4 v) {
  float a = sum_sw32(v[0], v[1]), b = sum_sw32(v[2], v[3]);
  float c = sum_sw16(a, b);
  return c + dppf<ROR8>(c);
}
__device__ __forceinline__ v4u tr8(const v4u* in, int jd) {
  v4u o;
#pragma unroll
  for (int u = 0; u < 4; ++u) o[u] = __builtin_amdgcn_perm(in[2 * u + 1][jd >> 1], in[2 * u][jd >> 1], (jd & 1) ? 0x07060302u : 0x05040100u);
  return o;
}
__device__ __forceinline__ v4u frag_from_lanes(float v) {
  unsigned p = pk2(v, dppf<SHL1>(v));
  return v4u{p, dppu<SHL2>(p), dppu<SHL4>(p), dppu<SHL6>(p)};
}
__device__ __forceinline__ float sum8(const LAS unsigned char* p) {
  const f32x4 a = *(const LAS f32x4*)p, b = *(const LAS f32x4*)(p + 16);
  const f32x4 c = a + b;
  const f32x2 d = f32x2{c[0], c[1]} + f32x2{c[2], c[3]};
  return d[0] + d[1];
}
__device__ __forceinline__ void half_sum3(float& a, float& b, float& c) {
#define DPP3(ctl) "v_add_f32_dpp %0, %0, %0 " ctl "\n\tv_add_f32_dpp %1, %1, %1 " ctl "\n\tv_add_f32_dpp %2, %2, %2 " ctl "\n\t"
  asm volatile("s_nop 1\n\t" DPP3("quad_perm:[1,0,3,2] row_mask:0xf bank_mask:0xf") DPP3("quad_perm:[2,3,0,1] row_mask:0xf bank_mask:0xf")
               DPP3("row_half_mirror row_mask:0xf bank_mask:0xf") DPP3("row_ror:8 row_mask:0xf bank_mask:0xf") DPP3("row_bcast:15 row_mask:0xa bank_mask:0xf")
               : "+v"(a), "+v"(b), "+v"(c));
}
#define BUDGET() unsigned long long bud_; \
    asm volatile("s_add_u32 %0, %0, 1\n\ts_cmp_lt_u32 %0, %2\n\ts_cselect_b64 %1, -1, 0" : "+s"(spins), "=s"(bud_) : "i"(SPIN_CAP) : "memory", "scc")
#define AGAIN(bad) ((__builtin_amdgcn_ballot_w64(bad) & bud_) != 0ull)
template <int OFF> __device__ __forceinline__ void arrive(unsigned zv) {
  unsigned one;
  asm volatile("v_mov_b32 %0, 1\n\ts_mov_b64 exec, 1\n\tds_add_u32 %1, %0 offset:%2\n\ts_mov_b64 exec, -1" : "=&v"(one) : "v"(zv), "i"(OFF) : "memory");
}
template <int OFF> __device__ __forceinline__ unsigned arrive_rtn(unsigned zv) {
  unsigned t, old;
  asm volatile("v_mov_b32 %0, 1\n\ts_mov_b64 exec, 1\n\tds_add_rtn_u32 %0, %2, %0 offset:%3\n\ts_mov_b64 exec, -1\n\ts_waitcnt lgkmcnt(0)\n\tv_readfirstlane_b32 %1, %0"
               : "=&v"(t), "=s"(old) : "v"(zv), "i"(OFF) : "memory");
  return old;
}
template <int OFF> __device__ __forceinline__ void lds_post(unsigned av, int sval) {
  unsigned t;
  asm volatile("v_mov_b32 %0, %2\n\tds_write_b32 %1, %0 offset:%3" : "=&v"(t) : "v"(av), "s"(sval), "i"(OFF) : "memory");
}
template <int OFF, int S = 16> __device__ __forceinline__ unsigned lds_wait(unsigned zv, unsigned n) {
  unsigned t, v;
  asm volatile("s_branch 2f\n1:\n\ts_sleep %5\n2:\n\tds_read_b32 %0, %2 offset:%4\n\ts_waitcnt lgkmcnt(0)\n\tv_readfirstlane_b32 %1, %0\n\ts_cmp_lt_u32 %1, %3\n\ts_cbranch_scc1 1b"
               : "=&v"(t), "=&s"(v) : "v"(zv), "s"(n), "i"(OFF), "i"(S) : "memory", "scc");
  return v;
}
template <int CNT, bool TAGGED>
__device__ __forceinline__ bool publish64(LAS unsigned char* L, unsigned zv, int red, const float (&acc)[8], int w, int lane, unsigned tag, rsrc_t rW, int so) {
  ((LAS float*)(L + red))[w * 64 + 8 * (lane & 7) + (lane >> 3)] = reduce8(acc, lane);
  const bool last = arrive_rtn<CNT>(zv) == 3u;
  if (last) {
    if (TAGGED) {
      const LAS f32x2* f = (const LAS f32x2*)(L + red) + (lane & 31);
      const f32x2 s = (f[0] + f[32]) + (f[64] + f[96]);
      if (lane < 32) st128(v4u{f2u(s[0]), tag, f2u(s[1]), tag}, rW, lane * 16, so);
    } else {
      const LAS f32x4* f = (const LAS f32x4*)(L + red) + (lane & 15);
      const f32x4 s = (f[0] + f[16]) + (f[32] + f[48]);
      if (lane < 16) st128(v4u{nosent(s[0]), nosent(s[1]), nosent(s[2]), nosent(s[3])}, rW, lane * 16, so);
    }
  }
  return last;
}
constexpr int OOB = 0x7FFF0000;
#define GATHER4(g, oa, ob, d, so) do { \
    _Pragma("unroll 1") for (;;) { \
      g[0] = ld128<AUX_SC1>(rW, oa, so); g[1] = ld128<AUX_SC1>(rW, (oa) + (d), so); g[2] = ld128<AUX_SC1>(rW, ob, so); g[3] = ld128<AUX_SC1>(rW, (ob) + (d), so); \
      if (!wave_any(max(max(max4(g[0]), max4(g[1])), max(max4(g[2]), max4(g[3]))) == SENT) || ++spins >= SPIN_CAP) break; \
      __builtin_amdgcn_s_sleep(16); CFENCE(); \
    } } while (0)
__device__ __forceinline__ void wait_clock(unsigned t) {
  while ((unsigned)(t - (unsigned)__builtin_amdgcn_s_memrealtime()) < 1000u) { }
  CFENCE();
}
__device__ __forceinline__ unsigned latest(unsigned ta) {
  const unsigned now = (unsigned)__builtin_amdgcn_s_memrealtime();
  unsigned age = now - ta;
  age = min(age, dppu<XOR1>(age)); age = min(age, dppu<XOR2>(age));
  return now - __builtin_amdgcn_readfirstlane(age);
}
__device__ __forceinline__ int kvswz(int row) { return ((row & 7) << 1) | (row >> 3); }
__device__ __forceinline__ f32x4 asf(v4u v) { return __builtin_bit_cast(f32x4, v); }

typedef unsigned s16u __attribute__((ext_vector_type(16)));
typedef unsigned s8u __attribute__((ext_vector_type(8)));
__global__ void __launch_bounds__(832, 4)
fused_kernel(const u16*, const u16*, const u16*, const u16*, const u16*, const u16*, const u16*, const u16*, const u16*, const u16*, const u16*, u16*, unsigned char*) {
  s16u ka; s8u kb; v2u kd;
  asm volatile("s_load_dwordx16 %0, %3, 0x0\n\ts_load_dwordx8 %1, %3, 0x40\n\ts_load_dwordx2 %2, %3, 0x60"
               : "={s[36:51]}"(ka), "={s[52:59]}"(kb), "={s[60:61]}"(kd) : "s"(__builtin_amdgcn_kernarg_segment_ptr()));
  __shared__ __attribute__((aligned(16))) unsigned char lds_raw[L_BYTES];
  LAS unsigned char* const L = (LAS unsigned char*)lds_raw;
  const int hw = __builtin_amdgcn_readfirstlane(threadIdx.x >> 6), lane = threadIdx.x & 63;
  const int w = hw < 5 ? hw + 8 : hw - 5, tid = w * 64 + lane;
  const int cb = (blockIdx.x & 7) * 32 + (blockIdx.x >> 3);
  const int grp = cb >> 6, sp = cb & 63, ri = cb >> 1, rj = ri >> 2;
  const bool isX = (cb & 1) == 0;
  const bool isY = !isX && (cb & 31) < 28;
  const bool isRed = isX && (ri & 3) == 0;
  const int r16 = lane & 15, g4 = lane >> 4, rg = lane >> 3;
  unsigned spins = 0, zv = 0;
  const unsigned long long pc = __builtin_amdgcn_s_getpc();
  const v4u pfc = ld128<0>(mk((const void*)(pc & ~255ull), w == 0 ? CODE_BYTES : 0), lane * 16 + (blockIdx.x >> 3) * 1024, 0);
  PIN(zv);
  if (threadIdx.x < 32) ((LAS unsigned*)(L + L_CNT))[lane] = 0u;
  int vo[4];
#pragma unroll
  for (int i = 0; i < 4; ++i) { vo[i] = g4 * 256 + (r16 ^ kvswz(4 * i + g4)) * 16; PIN(vo[i]); }
  asm volatile("s_waitcnt lgkmcnt(0)" : "+{s[36:51]}"(ka), "+{s[52:59]}"(kb), "+{s[60:61]}"(kd));
  if (hw == 0) {
    const v4i srd = {(int)ka[2], (int)ka[3] & 0xffff, 4 * SK * DH * 2, 0x00020000};
#pragma unroll
    for (int i = 0; i < UINIT; ++i)
      asm volatile("s_mov_b32 m0, %0\n\ts_nop 0\n\t"
                   "buffer_load_dwordx4 %1, %5, %6 offen nt lds\n\tbuffer_load_dwordx4 %2, %5, %6 offen offset:1024 nt lds\n\t"
                   "buffer_load_dwordx4 %3, %5, %6 offen offset:2048 nt lds\n\tbuffer_load_dwordx4 %4, %5, %6 offen offset:3072 nt lds"
                   ::"i"(L_RING + i * 4096), "v"(vo[0]), "v"(vo[1]), "v"(vo[2]), "v"(vo[3]), "s"(srd), "s"(cb * 262144 + i * 4096) : "memory");
  }
  wg_barrier();
#define KPTR(T, v, i) ((T)(((unsigned long long)v[(i) + 1] << 32) | v[i]))
  const u16 *x = KPTR(const u16*, ka, 0), *kc = KPTR(const u16*, ka, 2), *vc = KPTR(const u16*, ka, 4), *wq = KPTR(const u16*, ka, 6), *wo = KPTR(const u16*, ka, 8),
            *w1 = KPTR(const u16*, ka, 10), *b1 = KPTR(const u16*, ka, 12), *w2 = KPTR(const u16*, ka, 14), *b2 = KPTR(const u16*, kb, 0), *cnd = KPTR(const u16*, kb, 2),
            *wmod = KPTR(const u16*, kb, 4);
  u16* out = KPTR(u16*, kb, 6);
  unsigned char* ws = KPTR(unsigned char*, kd, 0);
  const int yj = (cb & 31) >> 2, c3 = 3 - (int)(ka[10] >> 7);
  const int yi = 2 * (cb < 224 ? 8 * (cb >> 5) + ((c3 + yj) & 7) : 8 * yj + ((c3 + 7) & 7)) + ((cb >> 1) & 1);
  const int U_V = isX ? 64 : yj == 0 ? 64 - SHIFT : yj == 7 ? 64 + SHIFT : 64, U_W = 2 * U_V, U_WEND = U_W + 16;
  const rsrc_t rW = mk(ws, WS_BYTES);
  const int mi = ri - rj - 1, gi = 2 * rj + (ri & 1);
  const bool isM = isX && !isRed, isG = isX && ((ri + 1) & 2);
  const int g3o = WS_G3 + mi * 256, g4o = WS_G4 + gi * 256;

  if (__builtin_expect(w == 8, 1)) {
    lds_post<L_BASE>(zv, (int)(unsigned)__builtin_amdgcn_s_memrealtime());
    int dead = TOFF;
    const unsigned pgaddr = L_PROG + (lane & 7) * 4;
    int u = UINIT, pub = 0, lp = UINIT * 4096, pg = (int)zv;
    asm volatile("" : "+s"(u), "+s"(pub), "+s"(lp), "+s"(dead));
#define RUN(src, bytes, so, step, uend, tau, A0, A1, A2, A3) do { \
      const unsigned long long sa = (unsigned long long)src; \
      const v4i srd = {(int)sa, (int)(sa >> 32) & 0xffff, bytes, 0x00020000}; \
      { \
        int t, t2, tv; \
        asm volatile( \
            "s_branch 1f\n" \
            "3:\n\t" \
            "ds_read_b32 %[pg], %[pa]\n\t" \
            "s_waitcnt lgkmcnt(0)\n\t" \
            "v_cmp_gt_i32 vcc, %[t], %[pg]\n\t" \
            "s_cbranch_vccz 1f\n\t" \
            "s_waitcnt vmcnt(0)\n\t" \
            "s_mov_b32 %[pub], %[u]\n\t" \
            "v_mov_b32 %[tv], %[pub]\n\t" \
            "ds_write_b32 %[zv], %[tv] offset:48\n\t" \
            "s_sleep 1\n\t" \
            "s_branch 3b\n" \
            "1:\n\t" \
            "s_sub_i32 %[t], %[u], 30\n\t" \
            "v_cmp_gt_i32 vcc, %[t], %[pg]\n\t" \
            "s_add_u32 m0, %[lp], 32768\n\t" \
            "s_cbranch_vccnz 3b\n\t" \
            "buffer_load_dwordx4 %[v0], %[srd], %[sof] offen nt lds\n\t" \
            "buffer_load_dwordx4 %[v1], %[srd], %[sof] offen offset:1024 nt lds\n\t" \
            "buffer_load_dwordx4 %[v2], %[srd], %[sof] offen offset:2048 nt lds\n\t" \
            "buffer_load_dwordx4 %[v3], %[srd], %[sof] offen offset:3072 nt lds\n\t" \
            "s_add_u32 m0, %[lp], 36864\n\t" \
            "s_add_u32 %[sof], %[sof], %[stp]\n\t" \
            "buffer_load_dwordx4 %[v0], %[srd], %[sof] offen nt lds\n\t" \
            "buffer_load_dwordx4 %[v1], %[srd], %[sof] offen offset:1024 nt lds\n\t" \
            "buffer_load_dwordx4 %[v2], %[srd], %[sof] offen offset:2048 nt lds\n\t" \
            "buffer_load_dwordx4 %[v3], %[srd], %[sof] offen offset:3072 nt lds\n\t" \
            "ds_read_b32 %[pg], %[pa]\n\t" \
            "ds_read_b32 %[tv], %[zv] offset:52\n\t" \
            "s_memrealtime vcc\n\t" \
            "s_add_u32 %[sof], %[sof], %[stp]\n\t" \
            "s_add_u32 %[lp], %[lp], 0x2000\n\t" \
            "s_and_b32 %[lp], %[lp], 0x1ffff\n\t" \
            "s_add_u32 %[u], %[u], 2\n\t" \
            "s_mul_i32 %[t2], %[pub], %[ta]\n\t" \
            "s_lshr_b32 %[t2], %[t2], 1\n\t" \
            "s_add_u32 %[t2], %[t2], %[dead]\n\t" \
            "s_sub_i32 %[t], %[u], " STR(A3) "\n\t" \
            "s_waitcnt vmcnt(4*" STR(A3) ") lgkmcnt(0)\n\t" \
            "v_readfirstlane_b32 vcc_hi, %[tv]\n\t" \
            "s_sub_u32 vcc_lo, vcc_lo, %[t2]\n\t" \
            "s_sub_u32 vcc_lo, vcc_lo, vcc_hi\n\t" \
            "s_cmp_gt_i32 vcc_lo, " STR(TH3) "\n\t" \
            "s_cbranch_scc1 2f\n\t" \
            "s_waitcnt vmcnt(4*" STR(A2) ")\n\t" \
            "s_sub_i32 %[t], %[u], " STR(A2) "\n\t" \
            "s_cmp_gt_i32 vcc_lo, " STR(TH2) "\n\t" \
            "s_cbranch_scc1 2f\n\t" \
            "s_waitcnt vmcnt(4*" STR(A1) ")\n\t" \
            "s_sub_i32 %[t], %[u], " STR(A1) "\n\t" \
            "s_cmp_gt_i32 vcc_lo, " STR(TH1) "\n\t" \
            "s_cbranch_scc1 2f\n\t" \
            "s_waitcnt vmcnt(4*" STR(A0) ")\n\t" \
            "s_sub_i32 %[t], %[u], " STR(A0) "\n" \
            "2:\n\t" \
            "s_max_i32 %[pub], %[pub], %[t]\n\t" \
            "v_mov_b32 %[tv], %[pub]\n\t" \
            "s_cmp_lt_i32 %[u], %[ue]\n\t" \
            "ds_write_b32 %[zv], %[tv] offset:48\n\t" \
            "s_cbranch_scc1 1b" \
            : [t] "=&s"(t), [tv] "=&v"(tv), [u] "+s"(u), [sof] "+s"(so), [lp] "+s"(lp), [t2] "=&s"(t2), [pub] "+s"(pub), [pg] "+v"(pg) \
            : [v0] "v"(vo[0]), [v1] "v"(vo[1]), [v2] "v"(vo[2]), [v3] "v"(vo[3]), [srd] "s"(srd), [stp] "s"(step), [ue] "s"(uend), \
              [pa] "v"(pgaddr), [zv] "v"(zv), [ta] "s"(tau), [dead] "s"(dead) \
            : "memory", "vcc", "scc"); \
        asm volatile("" : "+s"(u), "+s"(so), "+s"(lp), "+s"(pub)); \
        PIN(pg); \
      } \
    } while (0)
#define PUBLISH(n) do { pub = max(pub, n); lds_post<L_FULL>(zv, pub); } while (0)
    {
      const int tk = U_V == 64 ? TAU2K : U_V < 64 ? (64 * TAU2K + SHIFT * TAU2) / (64 - SHIFT) : (64 * TAU2K - SHIFT * TAU2) / (64 + SHIFT), tv2 = TAU2, own = min(U_V, 64);
      int so = cb * 262144 + UINIT * 4096;
#pragma unroll 1
      for (int ph = 0; ph < 2; ++ph) {
        if (ph == 1) { if (U_V <= 64) break; so = (cb - 28) * 262144 + (64 - SHIFT) * 4096; }
        RUN(kc, 4 * SK * DH * 2, so, 4096, (ph == 0 ? own : U_V), tk, 4, 5, 5, 8);
      }
      so = cb * 262144; dead += (tk - tv2) * (U_V / 2);
#pragma unroll 1
      for (int ph = 0; ph < 2; ++ph) {
        if (ph == 1) { if (U_V <= 64) break; so = (cb - 28) * 262144 + (64 - SHIFT) * 4096; }
        RUN(vc, 4 * SK * DH * 2, so, 4096, (ph == 0 ? U_V + own : U_W), tv2, 3, 4, 5, 8);
      }
    }
    if (!isY) {
      VM(32); PUBLISH(U_W - 8); VM(24); PUBLISH(U_W - 6); VM(16); PUBLISH(U_W - 4); VM(8); PUBLISH(U_W - 2); VM(0); PUBLISH(U_W);
    } else {
#pragma unroll
      for (int i = 0; i < 4; ++i) vo[i] = (4 * i + g4) * 2048 + r16 * 16 - 1024 * i;
      int so = yi * 65536;
      dead -= 1 << 28;
#pragma unroll 1
      for (int ph = 0; ph < 3; ++ph) {
        RUN(w2, FF * D * 2, so, 256, (ph == 0 ? U_W + WPRE : U_W + 8 * ph), 0, 4, 4, 4, 13);
        if (ph == 0) {
          VM(4 * WPRE + 24); PUBLISH(U_W - 6); VM(4 * WPRE + 16); PUBLISH(U_W - 4); VM(4 * WPRE + 8); PUBLISH(U_W - 2); VM(4 * WPRE); PUBLISH(U_W);
          lds_wait<C_PUB, 1>(zv, 8);
          dead += 1 << 29;
        } else {
          so = yi * 65536 + 32768;
        }
      }
      VM(16); PUBLISH(U_WEND - 4); VM(0); PUBLISH(U_WEND);
    }
    __builtin_amdgcn_endpgm();
  }
  if (__builtin_expect(w > 8, 1)) {
    const int s = w - 9, st = tid - 576;
    __builtin_amdgcn_s_setprio(3);
    const unsigned tstart = (unsigned)__builtin_amdgcn_s_memrealtime();
    const rsrc_t rC = mk(cnd, D * 2), rX = mk(x, D * 2);
    const int f1i = (cb >> 6) * 16 + (cb & 15), f1k = f1i >> 3, f1c = (f1i & 4) * 4 + ((cb >> 4) & 3) * 4 + (f1i & 3);
    const int g2o = WS_G2 + ((cb & 63) * 4 + (cb >> 6)) * 256;
    v4u f1w[4], f2w[8];
    {
      const rsrc_t rMod = mk(wmod, D * 6 * D * 2);
      const int vo = rg * 12288 + (lane & 7) * 16, so = (f1k * 128 + 32 * s) * 12288 + f1c * 128;
#pragma unroll
      for (int i = 0; i < 4; ++i) f1w[i] = ld128<AUX_NT>(rMod, vo, so + i * 8 * 12288);
    }
    const unsigned c1 = ld16(rC, (lane & 31) * 2, (f1k * 128 + 32 * s) * 2);
    const unsigned epoch = ld32<AUX_SC1>(rW, 0, WS_EPOCH);
    st128(v4u{SENT, SENT, SENT, SENT}, rW, st * 16, WS_SENT + cb * 8192);
    st128(v4u{SENT, SENT, SENT, SENT}, rW, st * 16, WS_SENT + cb * 8192 + 4096);
    st128(v4u{SENT, SENT, SENT, SENT}, rW, st < 16 ? g2o + st * 16 : st < 33 ? WS_U + cb * 272 + (st - 16) * 16 : (st >= 48 && st < 64 && isM) ? g3o + (st - 48) * 16 : (st >= 64 && st < 80 && isG) ? g4o + (st - 64) * 16 : OOB, 0);
    SB();
    const unsigned c3 = ld32<0>(rC, st * 4, (mi & 1) * 1024);
    const v4u xa = ld128<0>(rX, lane * 32, 0), xb = ld128<0>(rX, lane * 32, 16);
    const unsigned xh = ld32<0>(rX, (lane & 31) * 4, ((cb >> 6) * 256 + 64 * s) * 2);
#define F2LOAD() do { \
      const rsrc_t rQ = mk(wq, D * 4096 * 2); \
      const int vo = rg * 8192 + (lane & 7) * 16, so = ((cb >> 6) * 256 + 64 * s) * 8192 + (cb & 63) * 128; \
      _Pragma("unroll") for (int i = 0; i < 8; ++i) f2w[i] = ld128<AUX_NT>(rQ, vo, so + i * 8 * 8192); \
      SB(); } while (0)
    LAS float* const ex = (LAS float*)(L + L_F1 + 1024 + s * 256);
    const unsigned tag = epoch + 1u;
    {
      ex[(lane & 7) * 4 + ((lane >> 3) & 3)] = silu_bf(bflo(c1));
      CFENCE();
      const f32x4 sv = *(const LAS f32x4*)(ex + rg * 4);
      float acc[8] = {0, 0, 0, 0, 0, 0, 0, 0};
#pragma unroll
      for (int i = 0; i < 4; ++i) fma8(acc, f1w[i], sv[i]);
      VM(0);
      publish64<C_F1, true>(L, zv, L_F1, acc, s, lane, tag, rW, WS_G1 + (f1c * 8 + f1k) * 512);
      F2LOAD();
      if (s == 1 && (cb & 31) == 0) st64(v2u{tstart, tag}, rW, lane ? OOB : 0, WS_TS + (cb >> 5) * 128);
    }
    float ln_m, ln_r;
    {
      float v[16];
#pragma unroll
      for (int j = 0; j < 4; ++j) { v[2 * j] = bflo(xa[j]); v[2 * j + 1] = bfhi(xa[j]); v[8 + 2 * j] = bflo(xb[j]); v[9 + 2 * j] = bfhi(xb[j]); }
      float sm = 0.f;
#pragma unroll
      for (int j = 0; j < 16; ++j) sm += v[j];
      const float mean = wave_sum(sm) * (1.f / D);
      float q = 0.f;
#pragma unroll
      for (int j = 0; j < 16; ++j) { const float d = v[j] - mean; q = fmaf(d, d, q); }
      ln_m = rbf(mean); ln_r = ln_rstd(wave_sum(q) * (1.f / D));
    }
    {
      const int g_vo = (lane >> 5) * 65536 + (lane & 31) * 16, g_so = WS_G1 + ((cb >> 6) * 4 + s) * 4096;
      float s0 = 0.f, s1 = 0.f;
      bool bad = true;
      VM(0);
#pragma unroll 1
      for (;;) {
        const int o = bad ? g_vo : OOB;
        v4u g[8];
#pragma unroll
        for (int i = 0; i < 8; ++i) g[i] = ld128<AUX_SC1>(rW, o + i * 512, g_so);
        bool nb = false;
#pragma unroll
        for (int i = 0; i < 8; ++i) nb |= (g[i].y != tag) | (g[i].w != tag);
        const float a0 = ((u2f(g[0].x) + u2f(g[1].x)) + (u2f(g[2].x) + u2f(g[3].x))) + ((u2f(g[4].x) + u2f(g[5].x)) + (u2f(g[6].x) + u2f(g[7].x)));
        const float a1 = ((u2f(g[0].z) + u2f(g[1].z)) + (u2f(g[2].z) + u2f(g[3].z))) + ((u2f(g[4].z) + u2f(g[5].z)) + (u2f(g[6].z) + u2f(g[7].z)));
        s0 = bad ? a0 : s0; s1 = bad ? a1 : s1;
        bad = bad && nb;
        if (!wave_any(bad) || ++spins >= SPIN_CAP) break;
        __builtin_amdgcn_s_sleep(16); CFENCE();
      }
      s0 = rbf(s0); s1 = rbf(s1);
      const auto e0s = __builtin_amdgcn_permlane32_swap(f2u(s0), f2u(s0), false, false);
      const auto e1s = __builtin_amdgcn_permlane32_swap(f2u(s1), f2u(s1), false, false);
      const float h0 = rbf(rbf(rbf(rbf(bflo(xh) - ln_m) * ln_r) * rbf(1.f + u2f(e0s[1]))) + u2f(e0s[0]));
      const float h1 = rbf(rbf(rbf(rbf(bfhi(xh) - ln_m) * ln_r) * rbf(1.f + u2f(e1s[1]))) + u2f(e1s[0]));
      LAS float* hp = ex + ((2 * lane) & 7) * 8 + ((lane & 31) >> 2);
      hp[0] = h0; hp[8] = h1;
      CFENCE();
      const f32x4 ha = *(const LAS f32x4*)(ex + rg * 8), hb = *(const LAS f32x4*)(ex + rg * 8 + 4);
      float acc[8] = {0, 0, 0, 0, 0, 0, 0, 0};
#pragma unroll
      for (int i = 0; i < 8; ++i) fma8(acc, f2w[i], i < 4 ? ha[i & 3] : hb[i & 3]);
      publish64<C_F2, false>(L, zv, L_F2, acc, s, lane, tag, rW, g2o);
    }
    if (s == 1) {
      const v2u ts = ld64<AUX_SC1>(rW, (lane & 7) * 128, WS_TS);
      int d = (int)(ts.x - tstart);
      d += (int)dppu<XOR1>(d); d += (int)dppu<XOR2>(d); d += (int)dppu<XOR7>(d);
      if (!wave_any(ts.y != tag)) lds_post<L_BASE>(zv, (int)tstart + __builtin_amdgcn_readfirstlane(d >> 3));
    }
    {
      lds_wait<L_FULL>(zv, U_V - 24);
      lds_wait<C_F2, 1>(zv, 4);
      const int g_vo = (st >> 4) * 1024 + (st & 15) * 16;
      v4u g[4];
      GATHER4(g, g_vo, g_vo + 512, 256, WS_G2 + grp * 16384);
      const f32x4 q4 = (asf(g[0]) + asf(g[1])) + (asf(g[2]) + asf(g[3]));
      *(LAS v2u*)(L + L_Q + st * 8) = v2u{pk2(q4[0], q4[1]), pk2(q4[2], q4[3])};
      if (arrive_rtn<C_Q>(zv) == 3u && cb == 0 && lane == 0) __builtin_amdgcn_raw_buffer_store_b32(tag, rW, 0, WS_EPOCH, AUX_SC1);
      lds_wait<C_Q, 1>(zv, 4);
    }
    if (isM) {
      LAS float* const sx = (LAS float*)(L + L_H + s * 512);
#pragma unroll 1
      for (int j = 0; j < 2; ++j) {
        const int r = 2 * lane + j;
        sx[(r >> 5) * 32 + (r & 7) * 4 + ((r >> 3) & 3)] = silu_bf(j ? bfhi(c3) : bflo(c3));
      }
      CFENCE();
      const rsrc_t rMod = mk(wmod, D * 6 * D * 2);
      const int vo = rg * 12288 + (lane & 7) * 16;
      int so = ((mi & 1) * 512 + 128 * s) * 12288 + 4096 + (mi >> 1) * 128;
      float acc[8] = {0, 0, 0, 0, 0, 0, 0, 0};
#pragma unroll 1
      for (int j = 0; j < 2; ++j, so += 64 * 12288) {
        v4u mw[8];
#pragma unroll
        for (int i = 0; i < 8; ++i) mw[i] = ld128<AUX_NT>(rMod, vo, so + i * 8 * 12288);
        SB();
        const f32x4 sv = *(const LAS f32x4*)(sx + j * 64 + rg * 4), sw = *(const LAS f32x4*)(sx + j * 64 + 32 + rg * 4);
#pragma unroll
        for (int i = 0; i < 8; ++i) fma8(acc, mw[i], i < 4 ? sv[i & 3] : sw[i & 3]);
      }
      publish64<C_F3, false>(L, zv, L_F3, acc, s, lane, 0u, rW, g3o);
    }
    lds_wait<L_FULL>(zv, U_W - 40);
    if (s == 2) { const v4u t = ld128<0>(mk((const void*)(pc & ~255ull), CODE_BYTES), lane * 16 + (blockIdx.x >> 3) * 1024, 0); asm volatile("" ::"v"(t)); }
    if (isY) {
      const int va = 2 * 8192 + (st >> 4) * 512 + (st & 15) * 16, vb = va - 8192;
      v4u g[4];
      GATHER4(g, va, vb, 256, WS_G3);
      const f32x4 cr = asf(g[0]) + asf(g[1]), sr = asf(g[2]) + asf(g[3]);
      const v2u cp = {pk2(1.f + rbf(cr[0]), 1.f + rbf(cr[1])), pk2(1.f + rbf(cr[2]), 1.f + rbf(cr[3]))}, sq = {pk2(sr[0], sr[1]), pk2(sr[2], sr[3])};
      *(LAS v2u*)(L + L_CS + st * 8) = cp;
      *(LAS v2u*)(L + L_CS + 2048 + st * 8) = sq;
      float kc5[5] = {0, 0, 0, 0, 0};
#pragma unroll
      for (int j = 0; j < 4; ++j) {
        const float c = (j & 1) ? bfhi(cp[j >> 1]) : bflo(cp[j >> 1]), sv = (j & 1) ? bfhi(sq[j >> 1]) : bflo(sq[j >> 1]);
        kc5[0] += c; kc5[1] += sv; kc5[2] = fmaf(c, c, kc5[2]); kc5[3] = fmaf(c, sv, kc5[3]); kc5[4] = fmaf(sv, sv, kc5[4]);
      }
#pragma unroll
      for (int i = 0; i < 5; ++i) { const float t = wave_sum(kc5[i]); if (lane == 0) ((LAS float*)(L + L_F1 + 1536))[i * 4 + s] = t; }
      arrive<C_CS>(zv);
    } else if (isRed && s == 1) {
      const int e = 32 * rj + 4 * (lane & 7), va = (((lane >> 3) & 3) % 3) * 8192 + (e >> 6) * 512 + (e & 63) * 4;
      const unsigned xv = ld16(rX, (lane & 31) * 2, rj * 64), bv = ld16(mk(b2, D * 2), (lane & 31) * 2, rj * 64);
      v4u g[4];
      GATHER4(g, va, va, 256, WS_G3);
      const f32x4 m = asf(g[0]) + asf(g[1]);
      const float one = ((lane >> 3) & 3) == 2 ? 1.f : 0.f;
      if (lane < 32) *(LAS f32x4*)(L + L_CS + lane * 16) = f32x4{rbf(one + rbf(m[0])), rbf(one + rbf(m[1])), rbf(one + rbf(m[2])), rbf(one + rbf(m[3]))};
      ((LAS float*)(L + L_CS + 512))[lane] = bflo(lane < 32 ? xv : bv);
      arrive<C_CS>(zv);
    }
    if (s == 3) {
      lds_wait<L_FULL>(zv, U_W - CALAT);
      const unsigned b = ld32<AUX_SC1>(rW, ((r16 >> 1) * 32 + 9 + (r16 & 1)) * 128 + 64, WS_ST);
      int d = (int)(b - tstart);
      d += (int)dppu<XOR1>(d); d += (int)dppu<XOR2>(d); d += (int)dppu<XOR7>(d); d += (int)dppu<ROR8>(d);
      if (!wave_any(b == SENT)) lds_post<L_BASE>(zv, (int)tstart + __builtin_amdgcn_readfirstlane((d >> 4) - TOFF + PBIAS));
    }
    if (s == 0) {
      if (isX) {
        lds_wait<L_FULL>(zv, U_W - 24);
        v4u g;
        _Pragma("unroll 1") for (;;) {
          g = ld128<AUX_SC1>(rW, lane * 128, WS_ST + grp * 8192 + ((ri >> 3) & 3) * 16);
          if (!wave_any(max4(g) == SENT) || ++spins >= SPIN_CAP) break;
          __builtin_amdgcn_s_sleep(16); CFENCE();
        }
        const float e0 = ex2((u2f(g.x) - wave_max(u2f(g.x))) * LOG2E), e1 = ex2((u2f(g.z) - wave_max(u2f(g.z))) * LOG2E);
        *(LAS f32x2*)(L + L_H + lane * 8) = f32x2{e0 * rcp(wave_sum(e0 * u2f(g.y))), e1 * rcp(wave_sum(e1 * u2f(g.w)))};
        arrive<C_ML>(zv);
      }
      if (isY) {
        lds_wait<C_CS>(zv, 4);
        float kcs[5];
#pragma unroll
        for (int i = 0; i < 5; ++i) { const f32x4 t = *(const LAS f32x4*)(L + L_F1 + 1536 + i * 16); kcs[i] = ((t[0] + t[1]) + (t[2] + t[3])) * (1.f / D); PIN(kcs[i]); }
        int xs_vo = (lane & 31) * 128 + (lane >> 5) * 16, sc_wr = lane >= 48 ? L_ST : L_ST + 16;
        PIN(xs_vo); PIN(sc_wr);
        lds_wait<L_FULL, 4>(zv, U_WEND);
        {
          unsigned ta;
          _Pragma("unroll 1") do {
            CFENCE();
            ta = ld32<AUX_SC1>(rW, (lane & 3) * 128, WS_U + (cb >> 5) * U_REP + 8192);
          } while (wave_any(ta == SENT) && ++spins < SPIN_CAP);
          const unsigned tr = (latest(ta) + XC) | 1u;
          lds_post<C_TR>(zv, (int)tr);
          wait_clock(tr + 4);
        }
        v4u gx = ld128<AUX_SC1>(rW, xs_vo, WS_U + (cb >> 5) * U_REP + 4096);
        if (__builtin_expect(wave_any(max4(gx) == SENT), 0)) {
          _Pragma("unroll 1") do {
            CFENCE();
            gx = ld128<AUX_SC1>(rW, xs_vo, WS_U + (cb >> 5) * U_REP + 4096);
          } while (wave_any(max4(gx) == SENT) && ++spins < SPIN_CAP);
        }
        float suu = u2f(gx.x), suc = u2f(gx.y), sus = u2f(gx.z), m1, exx, su;
        half_sum3(suu, suc, sus);
        asm volatile("s_nop 1\n\tv_mov_b32_dpp %0, %3 row_bcast:31 row_mask:0xc bank_mask:0xf\n\tv_mov_b32_dpp %1, %4 row_bcast:31 row_mask:0xc bank_mask:0xf\n\t"
                     "v_mov_b32_dpp %2, %5 row_bcast:31 row_mask:0xc bank_mask:0xf" : "=&v"(m1), "=&v"(exx), "=&v"(su) : "v"(suu), "v"(suc), "v"(sus));
        const float r1 = ln_rstd(fmaxf(fmaf(-m1, m1, exx), 0.f)), m1b = rbf(m1);
        const float A1 = fmaf(-m1b, kcs[0], su), A2 = fmaf(m1b, fmaf(m1b, kcs[2], -2.f * suc), suu), A3 = 2.f * fmaf(-m1b, kcs[3], sus);
        const float m2 = fmaf(r1, A1, kcs[1]);
        const float e2 = fmaf(r1, fmaf(r1, A2, A3), kcs[4]);
        const float r2 = ln_rstd(fmaxf(fmaf(-m2, m2, e2), 0.f));
        const float n1 = -r1 * m1b, n2 = -rbf(m2);
        *(LAS f32x4*)(L + sc_wr) = f32x4{r1, n1, r2, n2};
        wg_barrier();
      }
    }
    if (isG) {
      LAS float* const sx = (LAS float*)(L + L_CS + s * 256);
      sx[(lane & 7) * 8 + (lane >> 3)] = silu_bf(bflo(ld16(rC, st * 2, (gi & 3) * 512)));
      CFENCE();
      const f32x4 sv = *(const LAS f32x4*)(sx + rg * 8), sw = *(const LAS f32x4*)(sx + rg * 8 + 4);
      const rsrc_t rMod = mk(wmod, D * 6 * D * 2);
      const int vo = rg * 12288 + (lane & 7) * 16, so = ((gi & 3) * 256 + 64 * s) * 12288 + 10240 + (gi >> 2) * 128;
      wg_barrier();
      __builtin_amdgcn_s_sleep(12);
      v4u mw[8];
#pragma unroll
      for (int i = 0; i < 8; ++i) mw[i] = ld128<AUX_NT>(rMod, vo, so + i * 8 * 12288);
      SB();
      float acc[8] = {0, 0, 0, 0, 0, 0, 0, 0};
#pragma unroll
      for (int i = 0; i < 8; ++i) fma8(acc, mw[i], i < 4 ? sv[i & 3] : sw[i & 3]);
      publish64<C_F4, false>(L, zv, L_F3, acc, s, lane, 0u, rW, g4o);
    }
    __builtin_amdgcn_endpgm();
  }
  {
    __builtin_amdgcn_s_setprio(2);
    LAS unsigned char* const prog = L + L_PROG + w * 4;
#define RELEASE(next) do { LGKM0(); *(volatile LAS int*)prog = (next); } while (0)
    const int efrag = 8 * g4 + (lane & 7);
    unsigned k_b;
    v4u wr[8], wf[8];
    const rsrc_t rR = mk(isX ? wo : w1, isX ? 4096 * D * 2 : isY ? D * FF * 2 : 0);
    const int r_vo = isX ? ((g4 >> 1) * 128 + (g4 & 1) * 8) * 2048 + r16 * 16 : g4 * 8 * (FF * 2) + r16 * 4;
    const int r_so = isX ? ((grp * 8 + 2 * ((ri >> 3) & 3)) * 128 + 16 * (ri & 7)) * 2048 + w * 256 : isY ? 128 * w * (FF * 2) + yi * 64 : 0;
#define W1LOAD(i) wr[(i) >> 2][(i) & 3] = ld32<0>(rR, r_vo, r_so + (((i) >> 3) * 32 + ((i) & 7)) * (FF * 2))
#define RLOAD(m) do { if (isX) { if ((m) < 8 - XLATE) wr[m] = ld128<AUX_NT>(rR, r_vo, r_so + (m) * 2048); } else { W1LOAD(4 * (m)); W1LOAD(4 * (m) + 1); W1LOAD(4 * (m) + 2); W1LOAD(4 * (m) + 3); } SB(); } while (0)
    k_b = ld16(mk(b1, isY ? FF * 2 : 0), efrag * 2 + yi * 64, 0);
    {
      const int ka = r16 * 256 + ((g4 ^ kvswz(r16)) * 16);
      v4u kp[4][4];
      RLOAD(0);
#pragma unroll
      for (int m = 0; m < 4; ++m) {
        const int u = 8 * m + w;
        lds_wait<L_FULL>(zv, u + 1);
#pragma unroll
        for (int ks = 0; ks < 4; ++ks) kp[m][ks] = *(const LAS v4u*)(L + L_RING + (u & 31) * 4096 + (ka ^ (ks * 64)));
        RELEASE(u + 8);
        if (m == 1) asm volatile("" : "+v"(k_b) : "v"(pfc));
        RLOAD(m + 1);
      }
      lds_wait<C_Q, 2>(zv, 4);
      v4u qf[4];
#pragma unroll
      for (int ks = 0; ks < 4; ++ks) qf[ks] = *(const LAS v4u*)(L + L_Q + (r16 & 7) * 256 + ks * 64 + g4 * 16);
      const int sw = L_S + (w >> 1) * 512 + (g4 * 8 + (r16 & 7)) * 16 + (w & 1) * 8;
#define QKSTEP(kf, m) do { \
        f32x4 d = {0, 0, 0, 0}; \
        _Pragma("unroll") for (int ks = 0; ks < 4; ++ks) d = mfma(kf[ks], qf[ks], d); \
        const unsigned a = pk2(d[0], d[1]), b = pk2(d[2], d[3]); \
        constexpr float SCALE = 0.08838834764831845f; \
        *(LAS v2u*)(L + sw + (m) * 2048) = v2u{pk2(bflo(a) * SCALE, bfhi(a) * SCALE), pk2(bflo(b) * SCALE, bfhi(b) * SCALE)}; } while (0)
#pragma unroll
      for (int m = 0; m < 4; ++m) QKSTEP(kp[m], m);
#define KSTEP(m) do { \
        const int u = 8 * (m) + w; \
        lds_wait<L_FULL, 4>(zv, u + 1); \
        v4u kf[4]; \
        _Pragma("unroll") for (int ks = 0; ks < 4; ++ks) kf[ks] = *(const LAS v4u*)(L + L_RING + (u & 31) * 4096 + (ka ^ (ks * 64))); \
        RELEASE(min(u + 8, U_V)); \
        QKSTEP(kf, m); } while (0)
#pragma unroll
      for (int m = 4; m < 7; ++m) {
        KSTEP(m);
        RLOAD(m + 1);
        if (m == 5 && w == 0) { const unsigned tv = ld32<AUX_NT>(mk(vc, 4 * SK * DH * 2), (lane & 3) * 65536, cb * 262144); asm volatile("" ::"v"(tv)); }
      }
      if (U_V < 64) *(LAS v2u*)(L + sw + 7 * 2048) = v2u{0xFF80FF80u, 0xFF80FF80u};
#pragma unroll 1
      for (int m = 7; 8 * m < U_V; ++m) KSTEP(m);
    }
    {
      LAS unsigned char* const sp_ = L + L_S + ((lane >> 5) * 4 + (w >> 1)) * 512 + (lane & 31) * 16 + (w & 1) * 8;
      CFENCE();
      float sc[20];
      float mx = -INFINITY;
      const bool fifth = U_V > 64 && lane < 32;
#pragma unroll
      for (int i = 0; i < 5; ++i) {
        v2u s = *(const LAS v2u*)(sp_ + i * 4096);
        if (i == 4 && !fifth) s = v2u{0xFF80FF80u, 0xFF80FF80u};
        sc[4 * i] = bflo(s.x); sc[4 * i + 1] = bfhi(s.x); sc[4 * i + 2] = bflo(s.y); sc[4 * i + 3] = bfhi(s.y);
      }
#pragma unroll
      for (int i = 0; i < 20; ++i) mx = fmaxf(mx, sc[i]);
      mx = max_hi3(mx);
      if (lane < 8) ((LAS float*)(L + L_ST))[w * 8 + lane] = mx;
      arrive<C_ST>(zv);
      lds_wait<C_ST, 1>(zv, 8);
      const float M = max_hi3(((const LAS float*)(L + L_ST))[lane]);
      float l = 0.f;
#pragma unroll
      for (int i = 0; i < 5; ++i) {
        float p[4];
#pragma unroll
        for (int j = 0; j < 4; ++j) { p[j] = ex2((sc[4 * i + j] - M) * LOG2E); l += p[j]; }
        if (i < 4 || fifth) *(LAS v2u*)(sp_ + i * 4096) = v2u{pk2(p[0], p[1]), pk2(p[2], p[3])};
      }
      l = sum_hi3(l);
      if (lane < 8) ((LAS float*)(L + L_ST + 256))[w * 8 + lane] = l;
      if (arrive_rtn<C_P>(zv) == 7u) {
        const float Ls = sum_hi3(((const LAS float*)(L + L_ST + 256))[lane]);
        v4u o = {nosent(M), nosent(Ls), nosent(dppf<SHL1>(M)), nosent(dppf<SHL1>(Ls))};
        const unsigned fu = lds_wait<L_FULL, 1>(zv, 0);
        if (lane == 8) o.x = nosentu((unsigned)__builtin_amdgcn_s_memrealtime() - 32 * TAU2K - (fu + 64 - U_W) * TAU2 / 2);
        if (lane < 10 && !(lane & 1)) st128(o, rW, lane * 8, WS_ST + cb * 128);
      }
    }
    int t_wr = L_F1 + (4 * (lane & 7) + (g4 & 1) * 2 + (g4 >> 1)) * 48 + w * 4;
    int t_rd = L_F1 + efrag * 48;
    int o_vo = (g4 & 1) ? OOB : (r16 >> 2) * STR_B + (r16 & 3) * 32 + (g4 >> 1) * 16;
    int o_so = (isX ? WS_PB + ri * 128 : WS_PD + yi * 128) + 4 * w * STR_B;
    int g_vo = tid * 16, g_so = WS_PA + ri * STR_A, kw_rd = L_H + (tid >> 2) * 4;
    PIN(t_wr); PIN(t_rd); PIN(o_vo); PIN(g_vo); PIN(kw_rd); asm volatile("" : "+s"(o_so), "+s"(g_so));
    unsigned tend;
    {
      const int vrow = 4 * g4 + (r16 >> 2);
      const int va = vrow * 256 + (((2 * w + ((lane >> 1) & 1)) ^ kvswz(vrow)) * 16) + (lane & 1) * 8;
      const int pa = L_S + (g4 * 8 + (r16 & 7)) * 16;
      unsigned full = 0;
#define VREAD(dst, t, S) do { \
        const int u_ = U_V + 2 * (t); \
        if (full < (unsigned)(u_ + 2)) full = lds_wait<L_FULL, S>(zv, u_ + 2); \
        LAS unsigned char* p_ = L + L_RING + (u_ & 31) * 4096 + va; \
        const v2u v0_ = __builtin_bit_cast(v2u, __builtin_amdgcn_ds_read_tr16_b64_v4i16((LAS v4s*)p_)); \
        const v2u v1_ = __builtin_bit_cast(v2u, __builtin_amdgcn_ds_read_tr16_b64_v4i16((LAS v4s*)(p_ + 4096))); \
        dst = v4u{v0_.x, v0_.y, v1_.x, v1_.y}; } while (0)
      lds_wait<C_P, 1>(zv, 8);
      f32x4 acc = {0, 0, 0, 0};
      int pa_vo = r16 < 8 ? (r16 >> 1) * 8 * STR_A + (r16 & 1) * 64 + g4 * 16 : OOB, pa_so = WS_PA + (grp * 32 + w) * STR_A + sp * 128;
      PIN(pa_vo); asm volatile("" : "+s"(pa_so));
#pragma unroll 1
      for (int t = 0; t < U_V / 2 - 4; t += 4) {
        v4u pf[4], vf[4];
#pragma unroll
        for (int i = 0; i < 4; ++i) pf[i] = *(const LAS v4u*)(L + pa + (t + i) * 512);
        VREAD(vf[3], t + 3, 4);
#pragma unroll
        for (int i = 0; i < 3; ++i) VREAD(vf[i], t + i, 2);
#pragma unroll
        for (int i = 0; i < 4; ++i) acc = mfma(vf[i], pf[i], acc);
        RELEASE(U_V + 2 * t + 8);
      }
#define PV1(from, to) _Pragma("unroll 1") for (int t = U_V / 2 - (from); t < U_V / 2 - (to); ++t) { \
        const v4u pfr = *(const LAS v4u*)(L + pa + t * 512); \
        v4u vf; \
        VREAD(vf, t, 1); \
        acc = mfma(vf, pfr, acc); }
      tend = lds_wait<L_BASE, 1>(zv, 0) + TOFF + 32 * (TAU2K + TAU2) - 100;
      PV1(4, XAT)
      if (isX) {
#pragma unroll
        for (int m = 8 - XLATE; m < 8; ++m) wr[m] = ld128<AUX_NT>(rR, r_vo, r_so + m * 2048);
      }
      SB();
      PV1(XAT, 0)
      st128(v4u{nosent(acc[0]), nosent(acc[1]), nosent(acc[2]), nosent(acc[3])}, rW, pa_vo, pa_so);
      SB();
      if (isY) arrive<C_PUB>(zv);
    }
    if (isX || isY) {
      if (!isX) st128(v4u{SENT, SENT, SENT, SENT}, rW, o_vo, o_so);
      float kC = 0.f, kZ = 0.f, kO = 0.f, kB = 0.f;
      if (isX) {
        wait_clock(tend + XA);
        v4u g = ld128<AUX_SC1>(rW, g_vo, g_so);
        SB();
        lds_wait<C_ML, 1>(zv, 1);
        float k_w = *(const LAS float*)(L + kw_rd);
        PIN(k_w);
#pragma unroll
        for (int jd = 0; jd < 8; ++jd) { wf[jd] = tr8(wr, jd); PIN(wf[jd]); }
        if (wave_any(max4(g) == SENT)) {
          _Pragma("unroll 1") for (;;) {
            g = ld128<AUX_SC1>(rW, g_vo, g_so);
            BUDGET();
            if (!AGAIN(max4(g) == SENT)) break;
          }
        }
        *(LAS float*)(L + t_wr) = reduce4(asf(g) * k_w);
      }
      int yrole = cb & 1;
      asm volatile("" : "+s"(yrole));
      if (yrole) {
        v4u f1[8];
#pragma unroll
        for (int ks = 0; ks < 4; ++ks)
#pragma unroll
          for (int u = 0; u < 4; ++u) {
            const unsigned lo = wr[2 * ks + (u >> 1)][(2 * u) & 3], hi = wr[2 * ks + (u >> 1)][(2 * u + 1) & 3];
            f1[2 * ks][u] = __builtin_amdgcn_perm(hi, lo, 0x05040100u); f1[2 * ks + 1][u] = __builtin_amdgcn_perm(hi, lo, 0x07060302u);
          }
        {
          lds_wait<C_CS>(zv, 4);
          f32x4 h0 = {0, 0, 0, 0}, h1 = {0, 0, 0, 0};
#pragma unroll
          for (int ks = 0; ks < 4; ++ks) {
            v4u a = *(const LAS v4u*)(L + L_CS + (r16 & 1) * 2048 + w * 256 + ks * 64 + g4 * 16);
            if (r16 & 2) a = v4u{0x3F803F80u, 0x3F803F80u, 0x3F803F80u, 0x3F803F80u};
            h0 = mfma(a, f1[2 * ks], h0); h1 = mfma(a, f1[2 * ks + 1], h1);
          }
          if (lane < 16) {
            LAS float* p = (LAS float*)(L + L_F2) + lane * 56 + w;
            p[0] = h0[0]; p[8] = h0[1]; p[16] = h0[2]; p[28] = h1[0]; p[36] = h1[1]; p[44] = h1[2];
          }
          arrive<C_PRE>(zv);
        }
        lds_wait<C_PRE, 1>(zv, 8);
        kC = sum8(L + L_F2 + efrag * 112); kZ = sum8(L + L_F2 + efrag * 112 + 32); kO = sum8(L + L_F2 + efrag * 112 + 64);
        kB = bflo(k_b);
        PIN(kC); PIN(kZ); PIN(kO); PIN(kB);
        lds_wait<L_FULL, 2>(zv, U_W + 8 + w + 1);
        {
          v4u w2r[8];
#pragma unroll
          for (int i = 0; i < 8; ++i) w2r[i] = *(const LAS v4u*)(L + L_RING + ((U_W + 8 * (g4 >> 1) + w) & 31) * 4096 + (g4 & 1) * 2048 + r16 * 16 + i * 256);
#pragma unroll
          for (int jd = 0; jd < 8; ++jd) { wf[jd] = tr8(w2r, jd); PIN(wf[jd]); }
        }
        int h_wr = L_F1 + r16 * 96 + w * 4, u_vo = (r16 & 1) * 64 + g4 * 16;
        int u_so = WS_U + (cb >> 5) * U_REP + w * 512;
        int u_v1 = u_vo + 128, u_v2 = u_vo + 256, u_v3 = u_vo + 384;
        PIN(h_wr); PIN(u_vo); PIN(u_v1); PIN(u_v2); PIN(u_v3); asm volatile("" : "+s"(u_so));
        lds_wait<L_FULL, 2>(zv, U_WEND);
        wait_clock(lds_wait<C_TR, 1>(zv, 1));
        f32x4 h0, h1;
        _Pragma("unroll 1") for (;;) {
          v4u ua[4];
          ua[0] = ld128<AUX_SC1>(rW, u_vo, u_so); ua[1] = ld128<AUX_SC1>(rW, u_v1, u_so); ua[2] = ld128<AUX_SC1>(rW, u_v2, u_so); ua[3] = ld128<AUX_SC1>(rW, u_v3, u_so);
          BUDGET();
          h0 = h1 = f32x4{0, 0, 0, 0};
#pragma unroll
          for (int ks = 0; ks < 4; ++ks) { h0 = mfma(ua[ks], f1[2 * ks], h0); h1 = mfma(ua[ks], f1[2 * ks + 1], h1); }
          SB();
          if (!AGAIN(max(max(max4(ua[0]), max4(ua[1])), max(max4(ua[2]), max4(ua[3]))) == SENT)) break;
        }
        asm volatile("" ::"v"(h0[2]), "v"(h0[3]), "v"(h1[2]), "v"(h1[3]));
        *(LAS float*)(L + h_wr) = h0[0] + h0[1]; *(LAS float*)(L + h_wr + 48) = h1[0] + h1[1];
      }
      wg_barrier();
      {
        f32x4 k = *(const LAS f32x4*)(L + L_ST);
        float v = sum8(L + t_rd);
        PIN(k);
        if (yrole) {
          const float t = rbf(rbf(k[2] * fmaf(k[0], v, fmaf(k[1], kC, fmaf(k[3], kO, kZ)))) + kB);
          constexpr float C1 = -2.f * 0.7978845608028654f * LOG2E;
          v = t * rcp(1.f + ex2(t * fmaf(t * t, C1 * 0.044715f, C1)));
        }
        const v4u af = frag_from_lanes(v);
        f32x4 d[8];
#pragma unroll
        for (int jd = 0; jd < 8; ++jd) d[(jd >> 1) + (jd & 1) * 4] = mfma(af, wf[(jd >> 1) + (jd & 1) * 4], f32x4{0, 0, 0, 0});
        SB();
        const bool hi = lane >= 32;
        st128(v4u{nosent(hi ? d[4][0] : d[0][0]), nosent(hi ? d[5][0] : d[1][0]), nosent(hi ? d[6][0] : d[2][0]), nosent(hi ? d[7][0] : d[3][0])}, rW, o_vo, o_so);
        asm volatile("" ::"v"(d[0]), "v"(d[1]), "v"(d[2]), "v"(d[3]), "v"(d[4]), "v"(d[5]), "v"(d[6]), "v"(d[7]));
      }
      if (isX && (ri & 31) == 3 && w == 7)
        __builtin_amdgcn_raw_buffer_store_b32(nosentu((unsigned)__builtin_amdgcn_s_memrealtime()), rW, lane < 8 ? lane * U_REP : OOB, WS_U + 8192 + grp * 128, AUX_SC1);
      if (isRed) {
        lds_wait<C_CS, 1>(zv, 1);
        const LAS float* kp = (const LAS float*)(L + L_CS) + (lane & 31);
        float k_ga = kp[0], k_s = kp[32], k_c = kp[64], k_x = kp[128], k_b2 = kp[160];
        if (lane < 32) k_s = k_c;
        int r_rd = L_F2 + (lane & 31) * 48;
        int c_vo = w == 0 ? (lane & 7) * U_REP + (lane >> 3) * 16 : (lane & 24) != 16 ? OOB : (lane & 7) * U_REP + 4096 + (lane >> 5) * 16;
        int f_vo = (lane & 39) ? OOB : lane * 2, g2_vo = g_vo + 8192;
        PIN(k_ga); PIN(k_s); PIN(k_c); PIN(k_x); PIN(k_b2); PIN(r_rd); PIN(c_vo); PIN(f_vo); PIN(g2_vo);
        const bool hi = lane >= 32;
        unsigned himask = hi ? ~0u : 0u;
        int c_so = WS_U + rj * 128;
        PIN(himask); asm volatile("" : "+s"(c_so));
        if (w < 2) st128(v4u{SENT, SENT, SENT, SENT}, rW, c_vo, WS_U + rj * 128);
        v4u ga, gb;
        _Pragma("unroll 1") for (;;) {
          ga = ld128<AUX_SC1>(rW, g_vo, WS_PB + rj * STR_B); gb = ld128<AUX_SC1>(rW, g2_vo, WS_PB + rj * STR_B);
          BUDGET();
          if (!AGAIN(max(max4(ga), max4(gb)) == SENT)) break;
        }
        *(LAS float*)(L + t_wr + 2048) = reduce4(asf(ga) + asf(gb));
        wg_barrier();
        const float x2 = rbf(k_x + rbf(k_ga * rbf(sum8(L + r_rd))));
        if (w < 2) {
          const float uu = x2 * k_c;
          v4u o;
          if (w == 0) {
            const float h = uu - u2f(f2u(rbf(uu)) & himask);
            const unsigned p = nosentu(pk2(h, dppf<SHL1>(h)));
            const unsigned qa = dppu<0x00>(p), qb = dppu<0xAA>(p);
            o[0] = (unsigned)__builtin_amdgcn_update_dpp((int)qa, (int)qa, 0x114, 0xf, 0xa, false);
            o[1] = (unsigned)__builtin_amdgcn_update_dpp((int)qb, (int)qb, 0x114, 0xf, 0xa, false);
            o[2] = (unsigned)__builtin_amdgcn_update_dpp((int)qa, (int)qa, 0x104, 0xf, 0x5, false);
            o[3] = (unsigned)__builtin_amdgcn_update_dpp((int)qb, (int)qb, 0x104, 0xf, 0x5, false);
          } else {
            const float z = (hi ? uu : x2) * (1.f / D);
            float s0 = z * (hi ? uu : 1.f), s1 = z * (hi ? k_c : x2), s2 = z * k_s;
            half_sum3(s0, s1, s2);
            o = v4u{nosent(s0), nosent(s1), nosent(s2), 0u};
          }
          st128(o, rW, c_vo, c_so);
        }
        int q_vo = w ? OOB : (rj >> 1) * 1024 + (lane >> 5) * 512 + (rj & 1) * 128 + (lane & 31) * 4;
        PIN(q_vo);
        if (w >= 6) g2_vo = OOB;
        wait_clock(latest(ld32<AUX_SC1>(rW, (lane & 3) * 128, WS_U + (cb >> 5) * U_REP + 8192)) + XC + XD);
        unsigned q0, q1;
        _Pragma("unroll 1") for (;;) {
          ga = ld128<AUX_SC1>(rW, g_vo, WS_PD + rj * STR_B); gb = ld128<AUX_SC1>(rW, g2_vo, WS_PD + rj * STR_B);
          q0 = ld32<AUX_SC1>(rW, q_vo, WS_G4); q1 = ld32<AUX_SC1>(rW, q_vo, WS_G4 + 256);
          BUDGET();
          if (!AGAIN(max(max(max4(ga), max4(gb)), max(q0, q1)) == SENT)) break;
        }
        *(LAS float*)(L + t_wr + 4096) = reduce4(asf(ga) + asf(gb));
        const float qs = u2f(q0) + u2f(q1);
        float k_gb = rbf(sum_sw32(qs, qs));
        PIN(k_gb);
        wg_barrier();
        if (w == 0) st128(frag_from_lanes(x2 + rbf(k_gb * rbf(rbf(sum8(L + r_rd + 2048)) + k_b2))), mk(out, D * 2), f_vo, rj * 64);
      }
    }
  }
}

}

extern "C" void kernel_launch(void* const* d_in, const int* in_sizes, int n_in, void* d_out, int out_size, void* d_ws, size_t ws_size,
                              hipStream_t stream) {
  (void)in_sizes; (void)n_in; (void)out_size; (void)ws_size;
  fused_kernel<<<dim3(256), dim3(832), 0, stream>>>(
      (const u16*)d_in[0], (const u16*)d_in[1], (const u16*)d_in[2], (const u16*)d_in[3], (const u16*)d_in[4], (const u16*)d_in[6],
      (const u16*)d_in[7], (const u16*)d_in[8], (const u16*)d_in[9], (const u16*)d_in[10], (const u16*)d_in[11], (u16*)d_out,
      (unsigned char*)d_ws);
}
```
